```python
import torch

def _softmax(x, axis=-1):
    return torch.softmax(x, dim=axis)
def _gelu(x):
    return torch.nn.functional.gelu(x, approximate="tanh")
def _silu(x):
    return torch.nn.functional.silu(x)
def _sigmoid(x):
    return torch.sigmoid(x)
def _layernorm(x, axis=-1, eps=1e-5):
    m = x.mean(dim=axis, keepdim=True)
    v = x.var(dim=axis, keepdim=True, unbiased=False)
    return (x - m) / torch.sqrt(v + eps)
def _scatter_add(tgt, idx, src, axis=0):
    return tgt.clone().index_add_(axis, idx, src)
def _segment_sum(src, seg, n):
    out = torch.zeros((n,) + src.shape[1:], dtype=src.dtype)
    return out.index_add_(0, seg, src)
def _pad(x, pw):
    flat = tuple(v for pair in reversed(pw) for v in pair)
    return torch.nn.functional.pad(x, flat)

kernel_name = 'recurrent_scan__sliding_block_cached__geglu_ffn__decode'

def setup_inputs():

    _w = (lambda a: (lambda t: t.to(torch.bfloat16) if t.is_floating_point() else t)(torch.as_tensor(a)))
    def _A_setup():
        B, Sq, D = 1, 1, 512
        Hq, Hkv, Sk, Dh = 32, 32, 32768, 128
        assert Hq % Hkv == 0, (Hq, Hkv)
        return {
            "x": _w((lambda *s: torch.randn(*s, dtype=torch.bfloat16) * 0.05)(B, Sq, D)),
            "k_cache": _w((lambda *s: torch.randn(*s, dtype=torch.bfloat16) * 1.6)(B, Hkv, Sk, Dh)),
            "v_cache": _w((lambda *s: torch.randn(*s, dtype=torch.bfloat16) * 0.2)(B, Hkv, Sk, Dh)),
            "wq": _w((lambda *s: torch.randn(*s, dtype=torch.bfloat16) * 1.6)(D, Hq * Dh) * (D ** -0.5)),
            "wo": _w((lambda *s: torch.randn(*s, dtype=torch.bfloat16) * 0.2)(Hq * Dh, D) * ((Hq * Dh) ** -0.5)),
        }
    _A_in = _A_setup()

    def _B_setup():
        B, S, D, F = 1, 1, 512, 1376
        return {
            "x": _w((lambda *s: torch.randn(*s, dtype=torch.bfloat16) * 0.05)(B, S, D)),
            "w_gate": _w((lambda *s: torch.randn(*s, dtype=torch.bfloat16) * 0.1)(D, F)),
            "w_up": _w((lambda *s: torch.randn(*s, dtype=torch.bfloat16) * 0.1)(D, F)),
            "w_down": _w((lambda *s: torch.randn(*s, dtype=torch.bfloat16) * 0.1)(F, D)),
        }
    _B_in = _B_setup()

    return {
        "A_x": _A_in["x"],
        "A_k_cache": _A_in["k_cache"],
        "A_v_cache": _A_in["v_cache"],
        "A_wq": _A_in["wq"],
        "A_wo": _A_in["wo"],
        "B_x": _B_in["x"],
        "B_w_gate": _B_in["w_gate"],
        "B_w_up": _B_in["w_up"],
        "B_w_down": _B_in["w_down"],
    }

def reference(A_x, A_k_cache, A_v_cache, A_wq, A_wo, B_x, B_w_gate, B_w_up, B_w_down):
    def _A_ref(x, k_cache, v_cache, wq, wo):
        B, Sq, D = x.shape
        Hq, Dh = 32, 128
        Hkv, Sk = k_cache.shape[1], k_cache.shape[2]
        W = min(4096, max(256, Sk // 4))
        rep = Hq // Hkv
        q = (lambda x, *d: x.permute(*d))((x @ wq).reshape(B, Sq, Hq, Dh), 0, 2, 1, 3)
        k = (lambda x, n, axis: torch.repeat_interleave(x, n, dim=axis))(k_cache[:, :, Sk - W:, :], rep, axis=1)
        v = (lambda x, n, axis: torch.repeat_interleave(x, n, dim=axis))(v_cache[:, :, Sk - W:, :], rep, axis=1)
        scores = torch.einsum("bhqd,bhkd->bhqk", q, k) * (Dh ** -0.5)
        p = _softmax(scores, axis=-1)
        o = torch.einsum("bhqk,bhkd->bhqd", p, v)
        return (lambda x, *d: x.permute(*d))(o, 0, 2, 1, 3).reshape(B, Sq, Hq * Dh) @ wo

    def _B_ref(x, w_gate, w_up, w_down):
        return (_gelu(x @ w_gate) * (x @ w_up)) @ w_down

    x = A_x
    xn = _layernorm(x, axis=-1)
    s = _A_ref(xn, A_k_cache, A_v_cache, A_wq, A_wo)
    g = _silu(_B_ref(xn, B_w_gate, B_w_up, B_w_down))
    return x + s * g
```

```cpp
#include <hip/hip_runtime.h>
#include <hip/hip_bf16.h>
#include <stdint.h>

typedef unsigned short u16;
constexpr int D = 512, HQ = 32, DH = 128, SK = 32768, W = 4096, F = 1376, NSPLIT = 8;

__device__ __forceinline__ float bf2f(u16 b) { return __uint_as_float((unsigned)b << 16); }
__device__ __forceinline__ float rbf(float f) {
  unsigned u = __float_as_uint(f); u += 0x7FFFu + ((u >> 16) & 1u); return __uint_as_float(u & 0xFFFF0000u); }
__device__ __forceinline__ u16 f2bf(float f) { return (u16)(__float_as_uint(rbf(f)) >> 16); }

__device__ void layernorm(const u16* x, float* xn, float* red) {
  int t = threadIdx.x;
  float a = bf2f(x[t]), b = bf2f(x[t + 256]);
  red[t] = a + b; __syncthreads();
  for (int s = 128; s > 0; s >>= 1) { if (t < s) red[t] += red[t + s]; __syncthreads(); }
  float meanf = red[0] / 512.f; __syncthreads();
  float da = a - meanf, db = b - meanf;
  red[t] = da * da + db * db; __syncthreads();
  for (int s = 128; s > 0; s >>= 1) { if (t < s) red[t] += red[t + s]; __syncthreads(); }
  float var = rbf(red[0] / 512.f); __syncthreads();
  float m = rbf(meanf);
  float den = rbf(sqrtf(rbf(var + 1e-5f)));
  xn[t] = rbf(rbf(a - m) / den); xn[t + 256] = rbf(rbf(b - m) / den);
  __syncthreads();
}

__global__ void k1(const u16* x, const u16* wq, const u16* wg, const u16* wu, float* q, float* h) {
  __shared__ float xn[512], red[256];
  layernorm(x, xn, red);
  int c = blockIdx.x * 256 + threadIdx.x;
  if (c < 4096) { float a = 0; for (int k = 0; k < D; ++k) a += xn[k] * bf2f(wq[(size_t)k * 4096 + c]); q[c] = rbf(a); }
  else { c -= 4096; if (c < F) { float g = 0, u = 0; for (int k = 0; k < D; ++k) { g += xn[k] * bf2f(wg[(size_t)k * F + c]); u += xn[k] * bf2f(wu[(size_t)k * F + c]); }
      g = rbf(g); u = rbf(u);
      float inner = 0.7978845608028654f * (g + 0.044715f * g * g * g);
      float ge = rbf(0.5f * g * (1.f + tanhf(inner)));
      h[c] = rbf(ge * u); } }
}
__global__ void k2(const u16* kc, const u16* vc, const float* q, float* part) {
  __shared__ float sq[128], p[512], red[256];
  int h = blockIdx.x / NSPLIT, s = blockIdx.x % NSPLIT, t = threadIdx.x;
  if (t < 128) sq[t] = q[h * 128 + t];
  __syncthreads();
  const u16* kb = kc + ((size_t)h * SK + (SK - W) + s * 512) * DH;
  const u16* vb = vc + ((size_t)h * SK + (SK - W) + s * 512) * DH;
  float mx = -INFINITY;
  for (int j = t; j < 512; j += 256) { float a = 0; for (int d = 0; d < DH; ++d) a += sq[d] * bf2f(kb[(size_t)j * DH + d]);
    a = rbf(rbf(a) * 0.08838834764831845f); p[j] = a; mx = fmaxf(mx, a); }
  red[t] = mx; __syncthreads();
  for (int k = 128; k > 0; k >>= 1) { if (t < k) red[t] = fmaxf(red[t], red[t + k]); __syncthreads(); }
  mx = red[0]; __syncthreads();
  float l = 0; for (int j = t; j < 512; j += 256) { float e = __expf(p[j] - mx); p[j] = e; l += e; }
  red[t] = l; __syncthreads();
  for (int k = 128; k > 0; k >>= 1) { if (t < k) red[t] += red[t + k]; __syncthreads(); }
  l = red[0];
  float* o = part + (size_t)blockIdx.x * 132;
  if (t < 128) { float a = 0; for (int j = 0; j < 512; ++j) a += p[j] * bf2f(vb[(size_t)j * DH + t]); o[4 + t] = a; }
  if (t == 0) { o[0] = mx; o[1] = l; }
}
__global__ void k3(const u16* x, const float* part, const float* h, const u16* wo, const u16* wd, u16* out) {
  __shared__ float o[4096], w[256], sh[F];
  int t = threadIdx.x;
  { int hh = t / 8; float M = -INFINITY, L = 0; for (int s = 0; s < 8; ++s) M = fmaxf(M, part[(hh * 8 + s) * 132]);
    for (int s = 0; s < 8; ++s) L += __expf(part[(hh * 8 + s) * 132] - M) * part[(hh * 8 + s) * 132 + 1];
    w[t] = __expf(part[t * 132] - M) / L; }
  for (int j = t; j < F; j += 256) sh[j] = h[j];
  __syncthreads();
  for (int j = t; j < 4096; j += 256) { int hh = j / 128, d = j % 128; float a = 0; for (int s = 0; s < 8; ++s) a += w[hh * 8 + s] * part[(hh * 8 + s) * 132 + 4 + d]; o[j] = rbf(a); }
  __syncthreads();
  int c = blockIdx.x * 256 + t;
  float s = 0; for (int k = 0; k < 4096; ++k) s += o[k] * bf2f(wo[(size_t)k * D + c]);
  float y = 0; for (int k = 0; k < F; ++k) y += sh[k] * bf2f(wd[(size_t)k * D + c]);
  s = rbf(s); y = rbf(y);
  float g = rbf(y / (1.f + __expf(-y)));
  out[c] = f2bf(bf2f(x[c]) + rbf(s * g));
}

extern "C" void kernel_launch(void* const* d_in, const int* in_sizes, int n_in,
                              void* d_out, int out_size, void* d_ws, size_t ws_size,
                              hipStream_t stream) {
  const u16* x = (const u16*)d_in[0]; const u16* kc = (const u16*)d_in[1]; const u16* vc = (const u16*)d_in[2];
  const u16* wq = (const u16*)d_in[3]; const u16* wo = (const u16*)d_in[4];
  const u16* wg = (const u16*)d_in[6]; const u16* wu = (const u16*)d_in[7]; const u16* wd = (const u16*)d_in[8];
  float* q = (float*)d_ws; float* h = q + 4096; float* part = h + 2048;
  k1<<<22, 256, 0, stream>>>(x, wq, wg, wu, q, h);
  k2<<<256, 256, 0, stream>>>(kc, vc, q, part);
  k3<<<2, 256, 0, stream>>>(x, part, h, wo, wd, (u16*)d_out);
}
```

```cpp
#include <hip/hip_runtime.h>
#include <hip/hip_bf16.h>
#include <stdint.h>
#include <stdio.h>

namespace {

typedef unsigned short u16;
typedef unsigned v4u __attribute__((ext_vector_type(4)));
typedef unsigned v2u __attribute__((ext_vector_type(2)));
typedef float v2f __attribute__((ext_vector_type(2)));
typedef __bf16 bf16x2 __attribute__((ext_vector_type(2)));
#define LAS __attribute__((address_space(3)))
typedef __amdgpu_buffer_rsrc_t rsrc_t;

constexpr int DM = 512, NH = 32, DH = 128, SK = 32768, WIN = 4096, FF = 1376;
constexpr int NWG = 256, NWAVE = 16, NTHR = NWAVE * 64;
constexpr int NGRP = 22;
constexpr int AUX_NT = 2, AUX_SC1 = 16;
constexpr int RW3 = 0x00020000;
constexpr unsigned SPIN_CAP = 1u << 16;

constexpr int WS_E = 0;
constexpr int WS_QP = 4096;
constexpr int WS_GU = WS_QP + 32 * 8 * 1024;
constexpr int WS_OP = WS_GU + 24 * 8 * 1024;
constexpr int WS_S2A = WS_OP + 32 * 8 * 2 * 1024;
constexpr int WS_S2F = WS_S2A + 8 * 64 * 512;
constexpr int WS_END = WS_S2F + 8 * 32 * 512;

constexpr int L_FLAG = 0;
constexpr int L_Q = 64;
constexpr int L_XN = L_Q + 256;
constexpr int L_S = L_XN + 3 * 1024;
constexpr int L_ML = L_S + 3 * 128;
constexpr int L_O = L_ML + 128;
constexpr int L_END = L_O + 16 * 512;

__device__ __forceinline__ float asf(unsigned u) { return __builtin_bit_cast(float, u); }
__device__ __forceinline__ unsigned asu(float f) { return __builtin_bit_cast(unsigned, f); }
__device__ __forceinline__ float lo16f(unsigned d) { return asf(d << 16); }
__device__ __forceinline__ float hi16f(unsigned d) { return asf(d & 0xffff0000u); }
__device__ __forceinline__ unsigned pk2(float lo, float hi) {
  bf16x2 r = {(__bf16)lo, (__bf16)hi};
  return __builtin_bit_cast(unsigned, r);
}
__device__ __forceinline__ float rbf(float f) { return lo16f(pk2(f, f)); }
__device__ __forceinline__ float dot2(unsigned a, unsigned b, float c) {
  return __builtin_amdgcn_fdot2_f32_bf16(__builtin_bit_cast(bf16x2, a), __builtin_bit_cast(bf16x2, b), c, false);
}
template <int CTRL> __device__ __forceinline__ unsigned dppu(unsigned x) { return __builtin_amdgcn_mov_dpp(x, CTRL, 0xf, 0xf, true); }
template <int CTRL> __device__ __forceinline__ float dppf(float x) { return asf(dppu<CTRL>(asu(x))); }
constexpr int XOR1 = 0xB1, XOR2 = 0x4E, HMIR = 0x141, ROR1 = 0x121, ROR2 = 0x122, ROR4 = 0x124, ROR8 = 0x128, BCAST = 0x150;
__device__ __forceinline__ float tr32(float a, float b) {
  auto s = __builtin_amdgcn_permlane32_swap(asu(a), asu(b), false, false);
  return asf(s[0]) + asf(s[1]);
}
__device__ __forceinline__ float tr16(float a, float b) {
  auto s = __builtin_amdgcn_permlane16_swap(asu(a), asu(b), false, false);
  return asf(s[0]) + asf(s[1]);
}
__device__ __forceinline__ float max32(float a) {
  auto s = __builtin_amdgcn_permlane32_swap(asu(a), asu(a), false, false);
  return fmaxf(asf(s[0]), asf(s[1]));
}
__device__ __forceinline__ float max16(float a) {
  auto s = __builtin_amdgcn_permlane16_swap(asu(a), asu(a), false, false);
  return fmaxf(asf(s[0]), asf(s[1]));
}
__device__ __forceinline__ float wave_sum(float x) {
  x += dppf<ROR8>(x); x += dppf<ROR4>(x); x += dppf<ROR2>(x); x += dppf<ROR1>(x);
  x = tr16(x, x);
  return tr32(x, x);
}
__device__ __forceinline__ float ex(float x) { return __builtin_amdgcn_exp2f(x * 1.4426950408889634f); }

__device__ __forceinline__ rsrc_t mk(const void* p, unsigned bytes) { return __builtin_amdgcn_make_buffer_rsrc((void*)p, 0, bytes, RW3); }
template <int AUX> __device__ __forceinline__ v4u ld16(rsrc_t r, int voff, int soff) { return __builtin_amdgcn_raw_buffer_load_b128(r, voff, soff, AUX); }
template <int AUX> __device__ __forceinline__ unsigned ld4(rsrc_t r, int voff, int soff) { return __builtin_amdgcn_raw_buffer_load_b32(r, voff, soff, AUX); }
__device__ __forceinline__ void st_gran(rsrc_t r, int voff, float a, float b, unsigned T) {
  v4u g = {asu(a), T, asu(b), ~T};
  __builtin_amdgcn_raw_buffer_store_b128(g, r, voff, 0, AUX_SC1);
}
__device__ __forceinline__ bool tag_ok(v4u g, unsigned T) { return g.y == T && g.w == ~T; }

#define SB() __builtin_amdgcn_sched_barrier(0)
#define CBAR() asm volatile("" ::: "memory")
__device__ __forceinline__ unsigned lds_ld(LAS unsigned char* L, int off) { return *(volatile LAS unsigned*)(L + off); }
__device__ __forceinline__ void lds_wait_ge(LAS unsigned char* L, int off, unsigned want) {
  for (unsigned s = 0; s < (SPIN_CAP << 4); ++s) {
    if ((unsigned)__builtin_amdgcn_readfirstlane(lds_ld(L, off)) >= want) break;
    __builtin_amdgcn_s_sleep(1);
  }
  CBAR();
}
__device__ __forceinline__ void lds_inc(LAS unsigned char* L, int off, int lane) {
  asm volatile("s_waitcnt lgkmcnt(0)" ::: "memory");
  if (lane == 0) __hip_atomic_fetch_add((LAS unsigned*)(L + off), 1u, __ATOMIC_RELAXED, __HIP_MEMORY_SCOPE_WORKGROUP);
  CBAR();
}

#define STAMP(i) do { } while (0)

__device__ __forceinline__ void layernorm_to_lds(v4u xv, LAS unsigned char* xb, int lane) {
  float f[8];
#pragma unroll
  for (int k = 0; k < 4; ++k) { f[2 * k] = lo16f(xv[k]); f[2 * k + 1] = hi16f(xv[k]); }
  float s = 0.f;
#pragma unroll
  for (int k = 0; k < 8; ++k) s += f[k];
  const float mean = wave_sum(s) * (1.f / 512.f);
  float v = 0.f;
#pragma unroll
  for (int k = 0; k < 8; ++k) { const float d = f[k] - mean; v = fmaf(d, d, v); }
  const float var = rbf(wave_sum(v) * (1.f / 512.f));
  const float m = rbf(mean);
  const float den = rbf(sqrtf(rbf(var + 1e-5f)));
  v4u o;
#pragma unroll
  for (int k = 0; k < 4; ++k) o[k] = pk2(rbf(f[2 * k] - m) / den, rbf(f[2 * k + 1] - m) / den);
  *(LAS v4u*)(xb + lane * 16) = o;
  asm volatile("s_waitcnt lgkmcnt(0)" ::: "memory");
}

__device__ __forceinline__ v2f reduce_r8(const float (&acc)[8]) {
  float t[4], u[2];
#pragma unroll
  for (int e = 0; e < 4; ++e) t[e] = tr32(acc[e], acc[e + 4]);
#pragma unroll
  for (int e = 0; e < 2; ++e) u[e] = tr16(t[e], t[e + 2]);
  v2f r; r.x = u[0] + dppf<ROR8>(u[0]); r.y = u[1] + dppf<ROR8>(u[1]);
  return r;
}

__device__ __forceinline__ void gemv64_pre(const v4u (&Wt)[8], unsigned (&lo)[4][4], unsigned (&hi)[4][4]) {
#pragma unroll
  for (int t = 0; t < 4; ++t)
#pragma unroll
    for (int k = 0; k < 4; ++k) {
      lo[t][k] = __builtin_amdgcn_perm(Wt[2 * t + 1][k], Wt[2 * t][k], 0x05040100u);
      hi[t][k] = __builtin_amdgcn_perm(Wt[2 * t + 1][k], Wt[2 * t][k], 0x07060302u);
    }
}
__device__ __forceinline__ v2f gemv64(unsigned pair, const unsigned (&lo)[4][4], const unsigned (&hi)[4][4], LAS unsigned char* sb, int lane) {
  if (lane < 32) {
    const int off = (2 * (lane & 3)) * 16 + (lane >> 2) * 2;
    *(LAS u16*)(sb + off) = (u16)(pair & 0xffffu);
    *(LAS u16*)(sb + off + 16) = (u16)(pair >> 16);
  }
  asm volatile("s_waitcnt lgkmcnt(0)" ::: "memory");
  const v4u Dv = *(LAS v4u*)(sb + (lane >> 3) * 16);
  float acc[8];
#pragma unroll
  for (int k = 0; k < 8; ++k) acc[k] = 0.f;
#pragma unroll
  for (int t = 0; t < 4; ++t)
#pragma unroll
    for (int k = 0; k < 4; ++k) {
      acc[2 * k] = dot2(lo[t][k], Dv[t], acc[2 * k]);
      acc[2 * k + 1] = dot2(hi[t][k], Dv[t], acc[2 * k + 1]);
    }
  return reduce_r8(acc);
}

struct Params {
  const u16* x; const u16* kc; const u16* vc; const u16* wq; const u16* wo;
  const u16* wg; const u16* wu; const u16* wd; u16* out; unsigned char* ws;
};

__global__ void __launch_bounds__(NTHR, 4) fused_decode_kernel(Params P) {
  __shared__ __attribute__((aligned(16))) unsigned char lds_raw[L_END];
  LAS unsigned char* L = (LAS unsigned char*)lds_raw;
  const int tid = threadIdx.x, lane = tid & 63;
  const int w = __builtin_amdgcn_readfirstlane(tid >> 6);
  const int b = blockIdx.x, xcd = b & 7, idx = b >> 3;
  const int h = xcd * 4 + (idx >> 3), j = idx & 7;
  const int g = (idx >> 3) * 8 + xcd;
  const bool ffn = g < NGRP;
  const int r = lane >> 4, c = lane & 15, r8 = lane >> 3, c8 = lane & 7;
  STAMP(0);
  if (tid < 4) ((LAS unsigned*)(L + L_FLAG))[tid] = 0u;
  asm volatile("s_waitcnt lgkmcnt(0)" ::: "memory");
  __builtin_amdgcn_s_barrier();
  CBAR();

  const rsrc_t rws = mk(P.ws, WS_END);
  unsigned T = 0;

  if (w < 4) {
    const unsigned E = ld4<AUX_SC1>(rws, WS_E, 0);
    if (w == 0) {
      const v4u xv = ld16<0>(mk(P.x, 1024), lane * 16, 0);
      const rsrc_t rq = mk(P.wq + (size_t)(64 * j) * 4096 + 128 * h, 64 * 8192);
      v4u A[16];
#pragma unroll
      for (int i = 0; i < 16; ++i) A[i] = ld16<AUX_NT>(rq, r * 8192 + c * 16, i * 32768);
      SB();
      T = __builtin_amdgcn_readfirstlane(E) + 1u;
      LAS unsigned char* xb = L + L_XN;
      layernorm_to_lds(xv, xb, lane);
      const unsigned slo = *(LAS u16*)(xb + (64 * j + 4 * c + r) * 2), shi = slo << 16;
      float acc[8];
#pragma unroll
      for (int k = 0; k < 8; ++k) acc[k] = 0.f;
#define QSTEP(i) { const unsigned a_ = dppu<BCAST + i>(slo), b_ = dppu<BCAST + i>(shi); \
      _Pragma("unroll") for (int k = 0; k < 4; ++k) { acc[2 * k] = dot2(A[i][k], a_, acc[2 * k]); acc[2 * k + 1] = dot2(A[i][k], b_, acc[2 * k + 1]); } }
      QSTEP(0) QSTEP(1) QSTEP(2) QSTEP(3) QSTEP(4) QSTEP(5) QSTEP(6) QSTEP(7)
      QSTEP(8) QSTEP(9) QSTEP(10) QSTEP(11) QSTEP(12) QSTEP(13) QSTEP(14) QSTEP(15)
#undef QSTEP
      float t[4];
#pragma unroll
      for (int e = 0; e < 4; ++e) t[e] = tr32(acc[e], acc[e + 4]);
      const float u0 = tr16(t[0], t[2]), u1 = tr16(t[1], t[3]);
      st_gran(rws, WS_QP + (h * 8 + j) * 1024 + (4 * c + r) * 16, u0, u1, T);
    } else if (w < 3) {
      if (ffn) {
        const v4u xv = ld16<0>(mk(P.x, 1024), lane * 16, 0);
        const u16* wsrc = (w == 1) ? P.wg : P.wu;
        const size_t e0 = (size_t)(64 * j) * FF + 64 * g;
        const rsrc_t rg = mk(wsrc + e0, (unsigned)((size_t)DM * FF - e0) * 2u);
        v4u A[8];
#pragma unroll
        for (int i = 0; i < 8; ++i) A[i] = ld16<AUX_NT>(rg, r8 * (FF * 2) + c8 * 16, i * 8 * FF * 2);
        SB();
        T = __builtin_amdgcn_readfirstlane(E) + 1u;
        LAS unsigned char* xb = L + L_XN + w * 1024;
        layernorm_to_lds(xv, xb, lane);
        float acc[8];
#pragma unroll
        for (int k = 0; k < 8; ++k) acc[k] = 0.f;
#pragma unroll
        for (int i = 0; i < 8; ++i) {
          const unsigned slo = *(LAS u16*)(xb + (64 * j + 8 * i + r8) * 2), shi = slo << 16;
#pragma unroll
          for (int k = 0; k < 4; ++k) { acc[2 * k] = dot2(A[i][k], slo, acc[2 * k]); acc[2 * k + 1] = dot2(A[i][k], shi, acc[2 * k + 1]); }
        }
        const v2f pr = reduce_r8(acc);
        if (!(lane & 8))
          st_gran(rws, WS_GU + (g * 8 + j) * 1024 + (w - 1) * 512 + (4 * c8 + 2 * (lane >> 5) + ((lane >> 4) & 1)) * 16, pr.x, pr.y, T);
      } else {
        T = __builtin_amdgcn_readfirstlane(E) + 1u;
      }
    } else {
      T = __builtin_amdgcn_readfirstlane(E) + 1u;
      float q0 = 0.f, q1 = 0.f;
      for (unsigned s = 0; s < SPIN_CAP; ++s) {
        CBAR();
        v4u G[8];
#pragma unroll
        for (int k = 0; k < 8; ++k) G[k] = ld16<AUX_SC1>(rws, lane * 16, WS_QP + (h * 8 + k) * 1024);
        bool ok = true;
        q0 = 0.f; q1 = 0.f;
#pragma unroll
        for (int k = 0; k < 8; ++k) { ok = ok && tag_ok(G[k], T); q0 += asf(G[k].x); q1 += asf(G[k].z); }
        if (__all(ok)) break;
      }
      *(LAS unsigned*)(L + L_Q + lane * 4) = pk2(q0, q1);
      asm volatile("s_waitcnt lgkmcnt(0)" ::: "memory");
      if (lane == 0) *(volatile LAS unsigned*)(L + L_FLAG) = 1u;
      CBAR();
    }
  }
  STAMP(1);

  {
    const size_t base = ((size_t)h * SK + (SK - WIN) + j * 512) * DH;
    const rsrc_t rk = mk(P.kc + base, 512 * 256), rv = mk(P.vc + base, 512 * 256);
    const int cx = (b * 37) & 127;
    const int voff = ((((w ^ cx) & 15) | (r << 7)) << 8) + c * 16;
    const int ix = cx >> 4;
    v4u K[8], V[8];
#pragma unroll
    for (int i = 0; i < 8; ++i) K[i] = ld16<AUX_NT>(rk, voff, ((i ^ ix) & 7) << 12);
#pragma unroll
    for (int i = 0; i < 8; ++i) V[i] = ld16<AUX_NT>(rv, voff, ((i ^ ix) & 7) << 12);
    SB();
    lds_inc(L, L_FLAG + 4, lane);
    STAMP(2);
    lds_wait_ge(L, L_FLAG, 1u);
    const v4u q = *(LAS v4u*)(L + L_Q + c * 16);
    STAMP(3);
    float x[8];
#pragma unroll
    for (int i = 0; i < 8; ++i) {
      float d = 0.f;
#pragma unroll
      for (int k = 0; k < 4; ++k) d = dot2(K[i][k], q[k], d);
      x[i] = d;
    }
    float y[4], z[2];
#pragma unroll
    for (int i = 0; i < 4; ++i) { const float t1 = x[i] + dppf<ROR8>(x[i]), t2 = x[i + 4] + dppf<ROR8>(x[i + 4]); y[i] = (c & 8) ? t2 : t1; }
#pragma unroll
    for (int i = 0; i < 2; ++i) { const float t1 = y[i] + dppf<HMIR>(y[i]), t2 = y[i + 2] + dppf<HMIR>(y[i + 2]); z[i] = (c & 4) ? t2 : t1; }
    const float t1 = z[0] + dppf<XOR2>(z[0]), t2 = z[1] + dppf<XOR2>(z[1]);
    const float u = (c & 2) ? t2 : t1;
    const float dotf = u + dppf<XOR1>(u);
    const float sc = rbf(rbf(dotf) * 0.08838834764831845f);
    float m = fmaxf(sc, dppf<ROR8>(sc)); m = fmaxf(m, dppf<ROR4>(m)); m = fmaxf(m, dppf<ROR2>(m));
    m = max32(max16(m));
    const float p = ex(sc - m);
    float l = (c & 1) ? 0.f : p;
    l = wave_sum(l);
    const unsigned shi = asu(rbf(p)), slo = shi >> 16;
    STAMP(4);
    float acc[8];
#pragma unroll
    for (int k = 0; k < 8; ++k) acc[k] = 0.f;
#define PVSTEP(i) { const unsigned a_ = dppu<BCAST + 2 * i>(slo), b_ = dppu<BCAST + 2 * i>(shi); \
    _Pragma("unroll") for (int k = 0; k < 4; ++k) { acc[2 * k] = dot2(V[i][k], a_, acc[2 * k]); acc[2 * k + 1] = dot2(V[i][k], b_, acc[2 * k + 1]); } }
    PVSTEP(0) PVSTEP(1) PVSTEP(2) PVSTEP(3) PVSTEP(4) PVSTEP(5) PVSTEP(6) PVSTEP(7)
#undef PVSTEP
    float t[4];
#pragma unroll
    for (int e = 0; e < 4; ++e) t[e] = tr32(acc[e], acc[e + 4]);
    v2f o2; o2.x = tr16(t[0], t[2]); o2.y = tr16(t[1], t[3]);
    *(LAS v2f*)(L + L_O + w * 512 + (4 * c + r) * 8) = o2;
    if (lane == 0) { v2f ml; ml.x = m; ml.y = l; *(LAS v2f*)(L + L_ML + w * 8) = ml; }
    lds_inc(L, L_FLAG + 8, lane);
  }
  STAMP(5);
  if (w >= 3) return;

  const int g32 = lane & 31, ph = lane >> 5;
  if (w == 1) {
    if (!ffn) return;
    float a0, a1, b0, b1;
    for (unsigned s = 0; s < SPIN_CAP; ++s) {
        CBAR();
      v4u G[4], U[4];
#pragma unroll
      for (int k = 0; k < 4; ++k) {
        G[k] = ld16<AUX_SC1>(rws, (ph * 1024) + g32 * 16, WS_GU + (g * 8 + 2 * k) * 1024);
        U[k] = ld16<AUX_SC1>(rws, (ph * 1024) + 512 + g32 * 16, WS_GU + (g * 8 + 2 * k) * 1024);
      }
      bool ok = true;
      a0 = a1 = b0 = b1 = 0.f;
#pragma unroll
      for (int k = 0; k < 4; ++k) {
        ok = ok && tag_ok(G[k], T) && tag_ok(U[k], T);
        a0 += asf(G[k].x); a1 += asf(G[k].z); b0 += asf(U[k].x); b1 += asf(U[k].z);
      }
      if (__all(ok)) break;
    }
    a0 = rbf(tr32(a0, a0)); a1 = rbf(tr32(a1, a1)); b0 = rbf(tr32(b0, b0)); b1 = rbf(tr32(b1, b1));
    auto gelu = [](float v) {
      const float inner = 0.7978845608028654f * (v + 0.044715f * v * v * v);
      const float th = 1.f - 2.f / (1.f + ex(2.f * inner));
      return rbf(0.5f * v * (1.f + th));
    };
    float h0 = gelu(a0) * b0, h1 = gelu(a1) * b1;
    if (64 * g + 2 * g32 >= FF) { h0 = 0.f; h1 = 0.f; }
    const unsigned hp = pk2(h0, h1);
    STAMP(6);
    lds_wait_ge(L, L_FLAG + 4, NWAVE);
    const size_t e0 = (size_t)(64 * g) * DM + 64 * j;
    const rsrc_t rd = mk(P.wd + e0, (unsigned)((size_t)FF * DM - e0) * 2u);
    v4u Wt[8];
#pragma unroll
    for (int i = 0; i < 8; ++i) Wt[i] = ld16<AUX_NT>(rd, r8 * 1024 + c8 * 16, i * 8192);
    unsigned lo[4][4], hi[4][4];
    gemv64_pre(Wt, lo, hi);
    const v2f pr = gemv64(hp, lo, hi, L + L_S + 128, lane);
    if (!(lane & 8))
      st_gran(rws, WS_S2F + (j * 32 + g) * 512 + (4 * c8 + 2 * (lane >> 5) + ((lane >> 4) & 1)) * 16, pr.x, pr.y, T);
    STAMP(7);
    return;
  }

  const int hf = w >> 1;
  const bool fin = (w == 0) && ((h & 3) == 0);
  unsigned xpair = 0;
  if (fin) xpair = ld4<0>(mk(P.x, 1024), (64 * j + 8 * (h >> 2) + 2 * (lane & 3)) * 2, 0);
  lds_wait_ge(L, L_FLAG + 4, NWAVE);
  const rsrc_t ro = mk(P.wo + (size_t)(128 * h + 64 * hf) * DM + 64 * j, 64 * 1024);
  v4u Wt[8];
#pragma unroll
  for (int i = 0; i < 8; ++i) Wt[i] = ld16<AUX_NT>(ro, r8 * 1024 + c8 * 16, i * 8192);
  SB();
  STAMP(6);
  lds_wait_ge(L, L_FLAG + 8, NWAVE);
  STAMP(7);
  {
    const v2f ml = *(LAS v2f*)(L + L_ML + (8 * ph + c8) * 8);
    v2f R[8];
#pragma unroll
    for (int k = 0; k < 8; ++k) R[k] = *(LAS v2f*)(L + L_O + (8 * ph + k) * 512 + (32 * hf + g32) * 8);
    float M = fmaxf(ml.x, dppf<XOR1>(ml.x)); M = fmaxf(M, dppf<XOR2>(M)); M = fmaxf(M, dppf<HMIR>(M));
    M = max32(M);
    const float e = ex(ml.x - M);
    float Ls = e * ml.y;
    Ls += dppf<XOR1>(Ls); Ls += dppf<XOR2>(Ls); Ls += dppf<HMIR>(Ls);
    Ls = tr32(Ls, Ls);
    float o0 = 0.f, o1 = 0.f;
#define CSTEP(k) { const float e_ = dppf<BCAST + k>(e); o0 = fmaf(e_, R[k].x, o0); o1 = fmaf(e_, R[k].y, o1); }
    CSTEP(0) CSTEP(1) CSTEP(2) CSTEP(3) CSTEP(4) CSTEP(5) CSTEP(6) CSTEP(7)
#undef CSTEP
    o0 = tr32(o0, o0); o1 = tr32(o1, o1);
    if (lane < 36) st_gran(rws, WS_OP + ((h * 8 + j) * 2 + hf) * 1024 + lane * 16, lane < 32 ? o0 : M, lane < 32 ? o1 : Ls, T);
  }
  STAMP(8);
  unsigned lo[4][4], hi[4][4];
  gemv64_pre(Wt, lo, hi);
  float on0, on1;
  for (unsigned s = 0; s < SPIN_CAP; ++s) {
        CBAR();
    v4u G[4];
#pragma unroll
    for (int k = 0; k < 4; ++k) G[k] = ld16<AUX_SC1>(rws, ph * 2048 + g32 * 16, WS_OP + ((h * 8 + 2 * k) * 2 + hf) * 1024);
    const v4u ML = ld16<AUX_SC1>(rws, (2 * (lane & 3) + ph) * 2048 + 512 + ((lane >> 2) & 3) * 16, WS_OP + (h * 8 * 2 + hf) * 1024);
    bool ok = tag_ok(ML, T);
#pragma unroll
    for (int k = 0; k < 4; ++k) ok = ok && tag_ok(G[k], T);
    const float mj = asf(ML.x), lj = asf(ML.z);
    float M = fmaxf(mj, dppf<XOR1>(mj)); M = fmaxf(M, dppf<XOR2>(M)); M = max32(M);
    const float e = ex(mj - M);
    float Ls = e * lj;
    Ls += dppf<XOR1>(Ls); Ls += dppf<XOR2>(Ls); Ls = tr32(Ls, Ls);
    const float wt = e / Ls;
    on0 = 0.f; on1 = 0.f;
#define GSTEP(k) { const float w_ = dppf<k * 0x55>(wt); on0 = fmaf(w_, asf(G[k].x), on0); on1 = fmaf(w_, asf(G[k].z), on1); }
    GSTEP(0) GSTEP(1) GSTEP(2) GSTEP(3)
#undef GSTEP
    if (__all(ok)) break;
  }
  STAMP(9);
  on0 = tr32(on0, on0); on1 = tr32(on1, on1);
  {
    const v2f pr = gemv64(pk2(on0, on1), lo, hi, L + L_S + w * 128, lane);
    if (!(lane & 8))
      st_gran(rws, WS_S2A + (j * 64 + 2 * h + hf) * 512 + (4 * c8 + 2 * (lane >> 5) + ((lane >> 4) & 1)) * 16, pr.x, pr.y, T);
  }
  STAMP(10);
  if (!fin) return;
  {
    const int kf = h >> 2;
    float s0, s1, y0, y1;
    const bool fv1 = (16 + (lane >> 2)) < NGRP;
    for (unsigned s = 0; s < SPIN_CAP; ++s) {
        CBAR();
      v4u A[4], Bf[2];
#pragma unroll
      for (int t = 0; t < 4; ++t) A[t] = ld16<AUX_SC1>(rws, (lane >> 2) * 512 + (lane & 3) * 16, WS_S2A + (j * 64 + 16 * t) * 512 + kf * 64);
#pragma unroll
      for (int t = 0; t < 2; ++t) Bf[t] = ld16<AUX_SC1>(rws, (lane >> 2) * 512 + (lane & 3) * 16, WS_S2F + (j * 32 + 16 * t) * 512 + kf * 64);
      bool ok = tag_ok(Bf[0], T) && (!fv1 || tag_ok(Bf[1], T));
      s0 = s1 = 0.f;
#pragma unroll
      for (int t = 0; t < 4; ++t) { ok = ok && tag_ok(A[t], T); s0 += asf(A[t].x); s1 += asf(A[t].z); }
      y0 = asf(Bf[0].x) + (fv1 ? asf(Bf[1].x) : 0.f);
      y1 = asf(Bf[0].z) + (fv1 ? asf(Bf[1].z) : 0.f);
      if (__all(ok)) break;
    }
    STAMP(11);
    auto red = [](float v) { v += dppf<ROR4>(v); v += dppf<ROR8>(v); v = tr16(v, v); return tr32(v, v); };
    s0 = rbf(red(s0)); s1 = rbf(red(s1)); y0 = rbf(red(y0)); y1 = rbf(red(y1));
    const float g0 = rbf(y0 / (1.f + ex(-y0))), g1 = rbf(y1 / (1.f + ex(-y1)));
    const unsigned o = pk2(lo16f(xpair) + rbf(s0 * g0), hi16f(xpair) + rbf(s1 * g1));
    if (lane < 4) ((unsigned*)P.out)[32 * j + 4 * kf + lane] = o;
    if (b == 0 && lane == 0) __builtin_amdgcn_raw_buffer_store_b32(T, rws, WS_E, 0, AUX_SC1);
    STAMP(12);
  }
}

}

extern "C" void kernel_launch(void* const* d_in, const int* in_sizes, int n_in, void* d_out, int out_size,
                              void* d_ws, size_t ws_size, hipStream_t stream) {
  Params p;
  p.x = (const u16*)d_in[0]; p.kc = (const u16*)d_in[1]; p.vc = (const u16*)d_in[2];
  p.wq = (const u16*)d_in[3]; p.wo = (const u16*)d_in[4];
  p.wg = (const u16*)d_in[6]; p.wu = (const u16*)d_in[7]; p.wd = (const u16*)d_in[8];
  p.out = (u16*)d_out; p.ws = (unsigned char*)d_ws;
  fprintf(stderr, "PTRS x %p k %p v %p wq %p wo %p bx %p wg %p wu %p wd %p out %p ws %p wssz %zu\n", d_in[0], d_in[1], d_in[2], d_in[3], d_in[4], d_in[5], d_in[6], d_in[7], d_in[8], d_out, d_ws, ws_size);
  fused_decode_kernel<<<NWG, NTHR, 0, stream>>>(p);
}
```

```cpp
#define LEAK 1
#include <hip/hip_runtime.h>
#include <hip/hip_bf16.h>
#include <stdint.h>

namespace {

typedef unsigned short u16;
typedef unsigned v4u __attribute__((ext_vector_type(4)));
typedef unsigned v2u __attribute__((ext_vector_type(2)));
typedef float v2f __attribute__((ext_vector_type(2)));
typedef __bf16 bf16x2 __attribute__((ext_vector_type(2)));
#define LAS __attribute__((address_space(3)))
typedef __amdgpu_buffer_rsrc_t rsrc_t;

constexpr int DM = 512, NH = 32, DH = 128, SK = 32768, WIN = 4096, FF = 1376;
constexpr int NWG = 256, NWAVE = 16, NTHR = NWAVE * 64;
constexpr int NGRP = 22;
constexpr int AUX_NT = 2, AUX_SC1 = 16;
constexpr int RW3 = 0x00020000;
constexpr unsigned SPIN_CAP = 1u << 16;

constexpr int WS_E = 0;
constexpr int WS_QP = 4096;
constexpr int WS_GU = WS_QP + 32 * 8 * 1024;
constexpr int WS_OP = WS_GU + 24 * 8 * 1024;
constexpr int WS_S2A = WS_OP + 32 * 8 * 2 * 1024;
constexpr int WS_S2F = WS_S2A + 8 * 64 * 512;
constexpr int WS_END = WS_S2F + 8 * 32 * 512;

constexpr int L_FLAG = 0;
constexpr int L_Q = 64;
constexpr int L_XN = L_Q + 256;
constexpr int L_S = L_XN + 3 * 1024;
constexpr int L_ML = L_S + 3 * 128;
constexpr int L_O = L_ML + 128;
constexpr int L_END = L_O + 16 * 512;

__device__ __forceinline__ float asf(unsigned u) { return __builtin_bit_cast(float, u); }
__device__ __forceinline__ unsigned asu(float f) { return __builtin_bit_cast(unsigned, f); }
__device__ __forceinline__ float lo16f(unsigned d) { return asf(d << 16); }
__device__ __forceinline__ float hi16f(unsigned d) { return asf(d & 0xffff0000u); }
__device__ __forceinline__ unsigned pk2(float lo, float hi) {
  bf16x2 r = {(__bf16)lo, (__bf16)hi};
  return __builtin_bit_cast(unsigned, r);
}
__device__ __forceinline__ float rbf(float f) { return lo16f(pk2(f, f)); }
__device__ __forceinline__ float dot2(unsigned a, unsigned b, float c) {
  return __builtin_amdgcn_fdot2_f32_bf16(__builtin_bit_cast(bf16x2, a), __builtin_bit_cast(bf16x2, b), c, false);
}
template <int CTRL> __device__ __forceinline__ unsigned dppu(unsigned x) { return __builtin_amdgcn_mov_dpp(x, CTRL, 0xf, 0xf, true); }
template <int CTRL> __device__ __forceinline__ float dppf(float x) { return asf(dppu<CTRL>(asu(x))); }
constexpr int XOR1 = 0xB1, XOR2 = 0x4E, HMIR = 0x141, ROR1 = 0x121, ROR2 = 0x122, ROR4 = 0x124, ROR8 = 0x128, BCAST = 0x150;
__device__ __forceinline__ float tr32(float a, float b) {
  auto s = __builtin_amdgcn_permlane32_swap(asu(a), asu(b), false, false);
  return asf(s[0]) + asf(s[1]);
}
__device__ __forceinline__ float tr16(float a, float b) {
  auto s = __builtin_amdgcn_permlane16_swap(asu(a), asu(b), false, false);
  return asf(s[0]) + asf(s[1]);
}
__device__ __forceinline__ float max32(float a) {
  auto s = __builtin_amdgcn_permlane32_swap(asu(a), asu(a), false, false);
  return fmaxf(asf(s[0]), asf(s[1]));
}
__device__ __forceinline__ float max16(float a) {
  auto s = __builtin_amdgcn_permlane16_swap(asu(a), asu(a), false, false);
  return fmaxf(asf(s[0]), asf(s[1]));
}
__device__ __forceinline__ float wave_sum(float x) {
  x += dppf<ROR8>(x); x += dppf<ROR4>(x); x += dppf<ROR2>(x); x += dppf<ROR1>(x);
  x = tr16(x, x);
  return tr32(x, x);
}
__device__ __forceinline__ float ex(float x) { return __builtin_amdgcn_exp2f(x * 1.4426950408889634f); }

__device__ __forceinline__ rsrc_t mk(const void* p, unsigned bytes) { return __builtin_amdgcn_make_buffer_rsrc((void*)p, 0, bytes, RW3); }
template <int AUX> __device__ __forceinline__ v4u ld16(rsrc_t r, int voff, int soff) { return __builtin_amdgcn_raw_buffer_load_b128(r, voff, soff, AUX); }
template <int AUX> __device__ __forceinline__ unsigned ld4(rsrc_t r, int voff, int soff) { return __builtin_amdgcn_raw_buffer_load_b32(r, voff, soff, AUX); }
__device__ __forceinline__ void st_gran(rsrc_t r, int voff, float a, float b, unsigned T) {
  v4u g = {asu(a), T, asu(b), ~T};
  __builtin_amdgcn_raw_buffer_store_b128(g, r, voff, 0, AUX_SC1);
}
__device__ __forceinline__ bool tag_ok(v4u g, unsigned T) { return g.y == T && g.w == ~T; }

#define SB() __builtin_amdgcn_sched_barrier(0)
#define CBAR() asm volatile("" ::: "memory")
__device__ __forceinline__ unsigned lds_ld(LAS unsigned char* L, int off) { return *(volatile LAS unsigned*)(L + off); }
__device__ __forceinline__ void lds_wait_ge(LAS unsigned char* L, int off, unsigned want) {
  for (unsigned s = 0; s < (SPIN_CAP << 4); ++s) {
    if ((unsigned)__builtin_amdgcn_readfirstlane(lds_ld(L, off)) >= want) break;
    __builtin_amdgcn_s_sleep(1);
  }
  CBAR();
}
__device__ __forceinline__ void lds_inc(LAS unsigned char* L, int off, int lane) {
  asm volatile("s_waitcnt lgkmcnt(0)" ::: "memory");
  if (lane == 0) __hip_atomic_fetch_add((LAS unsigned*)(L + off), 1u, __ATOMIC_RELAXED, __HIP_MEMORY_SCOPE_WORKGROUP);
  CBAR();
}

#define STAMP(i) do { if (i == 0 || i == 12) { SB(); unsigned long long t_ = __builtin_amdgcn_s_memrealtime(); if (b == 0 && w == 0 && lane == 0) ((unsigned long long*)(P.ws + (3 << 20)))[i == 0 ? 1 : 2] = t_; SB(); } } while (0)

__device__ __forceinline__ void layernorm_to_lds(v4u xv, LAS unsigned char* xb, int lane) {
  float f[8];
#pragma unroll
  for (int k = 0; k < 4; ++k) { f[2 * k] = lo16f(xv[k]); f[2 * k + 1] = hi16f(xv[k]); }
  float s = 0.f;
#pragma unroll
  for (int k = 0; k < 8; ++k) s += f[k];
  const float mean = wave_sum(s) * (1.f / 512.f);
  float v = 0.f;
#pragma unroll
  for (int k = 0; k < 8; ++k) { const float d = f[k] - mean; v = fmaf(d, d, v); }
  const float var = rbf(wave_sum(v) * (1.f / 512.f));
  const float m = rbf(mean);
  const float den = rbf(sqrtf(rbf(var + 1e-5f)));
  v4u o;
#pragma unroll
  for (int k = 0; k < 4; ++k) o[k] = pk2(rbf(f[2 * k] - m) / den, rbf(f[2 * k + 1] - m) / den);
  *(LAS v4u*)(xb + lane * 16) = o;
  asm volatile("s_waitcnt lgkmcnt(0)" ::: "memory");
}

__device__ __forceinline__ v2f reduce_r8(const float (&acc)[8]) {
  float t[4], u[2];
#pragma unroll
  for (int e = 0; e < 4; ++e) t[e] = tr32(acc[e], acc[e + 4]);
#pragma unroll
  for (int e = 0; e < 2; ++e) u[e] = tr16(t[e], t[e + 2]);
  v2f r; r.x = u[0] + dppf<ROR8>(u[0]); r.y = u[1] + dppf<ROR8>(u[1]);
  return r;
}

__device__ __forceinline__ void gemv64_pre(const v4u (&Wt)[8], unsigned (&lo)[4][4], unsigned (&hi)[4][4]) {
#pragma unroll
  for (int t = 0; t < 4; ++t)
#pragma unroll
    for (int k = 0; k < 4; ++k) {
      lo[t][k] = __builtin_amdgcn_perm(Wt[2 * t + 1][k], Wt[2 * t][k], 0x05040100u);
      hi[t][k] = __builtin_amdgcn_perm(Wt[2 * t + 1][k], Wt[2 * t][k], 0x07060302u);
    }
}
__device__ __forceinline__ v2f gemv64(unsigned pair, const unsigned (&lo)[4][4], const unsigned (&hi)[4][4], LAS unsigned char* sb, int lane) {
  if (lane < 32) {
    const int off = (2 * (lane & 3)) * 16 + (lane >> 2) * 2;
    *(LAS u16*)(sb + off) = (u16)(pair & 0xffffu);
    *(LAS u16*)(sb + off + 16) = (u16)(pair >> 16);
  }
  asm volatile("s_waitcnt lgkmcnt(0)" ::: "memory");
  const v4u Dv = *(LAS v4u*)(sb + (lane >> 3) * 16);
  float acc[8];
#pragma unroll
  for (int k = 0; k < 8; ++k) acc[k] = 0.f;
#pragma unroll
  for (int t = 0; t < 4; ++t)
#pragma unroll
    for (int k = 0; k < 4; ++k) {
      acc[2 * k] = dot2(lo[t][k], Dv[t], acc[2 * k]);
      acc[2 * k + 1] = dot2(hi[t][k], Dv[t], acc[2 * k + 1]);
    }
  return reduce_r8(acc);
}

struct Params {
  const u16* x; const u16* kc; const u16* vc; const u16* wq; const u16* wo;
  const u16* wg; const u16* wu; const u16* wd; u16* out; unsigned char* ws;
};

__global__ void __launch_bounds__(NTHR, 4) fused_decode_kernel(Params P) {
  __shared__ __attribute__((aligned(16))) unsigned char lds_raw[L_END];
  LAS unsigned char* L = (LAS unsigned char*)lds_raw;
  const int tid = threadIdx.x, lane = tid & 63;
  const int w = __builtin_amdgcn_readfirstlane(tid >> 6);
  const int b = blockIdx.x, xcd = b & 7, idx = b >> 3;
  const int h = xcd * 4 + (idx >> 3), j = idx & 7;
  const int g = (idx >> 3) * 8 + xcd;
  const bool ffn = g < NGRP;
  const int r = lane >> 4, c = lane & 15, r8 = lane >> 3, c8 = lane & 7;
  STAMP(0);
  if (tid < 4) ((LAS unsigned*)(L + L_FLAG))[tid] = 0u;
  asm volatile("s_waitcnt lgkmcnt(0)" ::: "memory");
  __builtin_amdgcn_s_barrier();
  CBAR();

  const rsrc_t rws = mk(P.ws, WS_END);
  unsigned T = 0;

  if (w < 4) {
    const unsigned E = ld4<AUX_SC1>(rws, WS_E, 0);
    if (w == 0) {
      const v4u xv = ld16<0>(mk(P.x, 1024), lane * 16, 0);
      const rsrc_t rq = mk(P.wq + (size_t)(64 * j) * 4096 + 128 * h, 64 * 8192);
      v4u A[16];
#pragma unroll
      for (int i = 0; i < 16; ++i) A[i] = ld16<AUX_NT>(rq, r * 8192 + c * 16, i * 32768);
      SB();
      T = __builtin_amdgcn_readfirstlane(E) + 1u;
      LAS unsigned char* xb = L + L_XN;
      layernorm_to_lds(xv, xb, lane);
      const unsigned slo = *(LAS u16*)(xb + (64 * j + 4 * c + r) * 2), shi = slo << 16;
      float acc[8];
#pragma unroll
      for (int k = 0; k < 8; ++k) acc[k] = 0.f;
#define QSTEP(i) { const unsigned a_ = dppu<BCAST + i>(slo), b_ = dppu<BCAST + i>(shi); \
      _Pragma("unroll") for (int k = 0; k < 4; ++k) { acc[2 * k] = dot2(A[i][k], a_, acc[2 * k]); acc[2 * k + 1] = dot2(A[i][k], b_, acc[2 * k + 1]); } }
      QSTEP(0) QSTEP(1) QSTEP(2) QSTEP(3) QSTEP(4) QSTEP(5) QSTEP(6) QSTEP(7)
      QSTEP(8) QSTEP(9) QSTEP(10) QSTEP(11) QSTEP(12) QSTEP(13) QSTEP(14) QSTEP(15)
#undef QSTEP
      float t[4];
#pragma unroll
      for (int e = 0; e < 4; ++e) t[e] = tr32(acc[e], acc[e + 4]);
      const float u0 = tr16(t[0], t[2]), u1 = tr16(t[1], t[3]);
      st_gran(rws, WS_QP + (h * 8 + j) * 1024 + (4 * c + r) * 16, u0, u1, T);
    } else if (w < 3) {
      if (ffn) {
        const v4u xv = ld16<0>(mk(P.x, 1024), lane * 16, 0);
        const u16* wsrc = (w == 1) ? P.wg : P.wu;
        const size_t e0 = (size_t)(64 * j) * FF + 64 * g;
        const rsrc_t rg = mk(wsrc + e0, (unsigned)((size_t)DM * FF - e0) * 2u);
        v4u A[8];
#pragma unroll
        for (int i = 0; i < 8; ++i) A[i] = ld16<AUX_NT>(rg, r8 * (FF * 2) + c8 * 16, i * 8 * FF * 2);
        SB();
        T = __builtin_amdgcn_readfirstlane(E) + 1u;
        LAS unsigned char* xb = L + L_XN + w * 1024;
        layernorm_to_lds(xv, xb, lane);
        float acc[8];
#pragma unroll
        for (int k = 0; k < 8; ++k) acc[k] = 0.f;
#pragma unroll
        for (int i = 0; i < 8; ++i) {
          const unsigned slo = *(LAS u16*)(xb + (64 * j + 8 * i + r8) * 2), shi = slo << 16;
#pragma unroll
          for (int k = 0; k < 4; ++k) { acc[2 * k] = dot2(A[i][k], slo, acc[2 * k]); acc[2 * k + 1] = dot2(A[i][k], shi, acc[2 * k + 1]); }
        }
        const v2f pr = reduce_r8(acc);
        if (!(lane & 8))
          st_gran(rws, WS_GU + (g * 8 + j) * 1024 + (w - 1) * 512 + (4 * c8 + 2 * (lane >> 5) + ((lane >> 4) & 1)) * 16, pr.x, pr.y, T);
      } else {
        T = __builtin_amdgcn_readfirstlane(E) + 1u;
      }
    } else {
      T = __builtin_amdgcn_readfirstlane(E) + 1u;
      float q0 = 0.f, q1 = 0.f;
      for (unsigned s = 0; s < SPIN_CAP; ++s) {
        CBAR();
        v4u G[8];
#pragma unroll
        for (int k = 0; k < 8; ++k) G[k] = ld16<AUX_SC1>(rws, lane * 16, WS_QP + (h * 8 + k) * 1024);
        bool ok = true;
        q0 = 0.f; q1 = 0.f;
#pragma unroll
        for (int k = 0; k < 8; ++k) { ok = ok && tag_ok(G[k], T); q0 += asf(G[k].x); q1 += asf(G[k].z); }
        if (__all(ok)) break;
      }
      *(LAS unsigned*)(L + L_Q + lane * 4) = pk2(q0, q1);
      asm volatile("s_waitcnt lgkmcnt(0)" ::: "memory");
      if (lane == 0) *(volatile LAS unsigned*)(L + L_FLAG) = 1u;
      CBAR();
    }
  }
  STAMP(1);

  {
    const size_t base = ((size_t)h * SK + (SK - WIN) + j * 512) * DH;
    const rsrc_t rk = mk(P.kc + base, 512 * 256), rv = mk(P.vc + base, 512 * 256);
    const int cx = (b * 37) & 127;
    const int voff = ((((w ^ cx) & 15) | (r << 7)) << 8) + c * 16;
    const int ix = cx >> 4;
    v4u K[8], V[8];
#pragma unroll
    for (int i = 0; i < 8; ++i) K[i] = ld16<AUX_NT>(rk, voff, ((i ^ ix) & 7) << 12);
#pragma unroll
    for (int i = 0; i < 8; ++i) V[i] = ld16<AUX_NT>(rv, voff, ((i ^ ix) & 7) << 12);
    SB();
    lds_inc(L, L_FLAG + 4, lane);
    STAMP(2);
    lds_wait_ge(L, L_FLAG, 1u);
    const v4u q = *(LAS v4u*)(L + L_Q + c * 16);
    STAMP(3);
    float x[8];
#pragma unroll
    for (int i = 0; i < 8; ++i) {
      float d = 0.f;
#pragma unroll
      for (int k = 0; k < 4; ++k) d = dot2(K[i][k], q[k], d);
      x[i] = d;
    }
    float y[4], z[2];
#pragma unroll
    for (int i = 0; i < 4; ++i) { const float t1 = x[i] + dppf<ROR8>(x[i]), t2 = x[i + 4] + dppf<ROR8>(x[i + 4]); y[i] = (c & 8) ? t2 : t1; }
#pragma unroll
    for (int i = 0; i < 2; ++i) { const float t1 = y[i] + dppf<HMIR>(y[i]), t2 = y[i + 2] + dppf<HMIR>(y[i + 2]); z[i] = (c & 4) ? t2 : t1; }
    const float t1 = z[0] + dppf<XOR2>(z[0]), t2 = z[1] + dppf<XOR2>(z[1]);
    const float u = (c & 2) ? t2 : t1;
    const float dotf = u + dppf<XOR1>(u);
    const float sc = rbf(rbf(dotf) * 0.08838834764831845f);
    float m = fmaxf(sc, dppf<ROR8>(sc)); m = fmaxf(m, dppf<ROR4>(m)); m = fmaxf(m, dppf<ROR2>(m));
    m = max32(max16(m));
    const float p = ex(sc - m);
    float l = (c & 1) ? 0.f : p;
    l = wave_sum(l);
    const unsigned shi = asu(rbf(p)), slo = shi >> 16;
    STAMP(4);
    float acc[8];
#pragma unroll
    for (int k = 0; k < 8; ++k) acc[k] = 0.f;
#define PVSTEP(i) { const unsigned a_ = dppu<BCAST + 2 * i>(slo), b_ = dppu<BCAST + 2 * i>(shi); \
    _Pragma("unroll") for (int k = 0; k < 4; ++k) { acc[2 * k] = dot2(V[i][k], a_, acc[2 * k]); acc[2 * k + 1] = dot2(V[i][k], b_, acc[2 * k + 1]); } }
    PVSTEP(0) PVSTEP(1) PVSTEP(2) PVSTEP(3) PVSTEP(4) PVSTEP(5) PVSTEP(6) PVSTEP(7)
#undef PVSTEP
    float t[4];
#pragma unroll
    for (int e = 0; e < 4; ++e) t[e] = tr32(acc[e], acc[e + 4]);
    v2f o2; o2.x = tr16(t[0], t[2]); o2.y = tr16(t[1], t[3]);
    *(LAS v2f*)(L + L_O + w * 512 + (4 * c + r) * 8) = o2;
    if (lane == 0) { v2f ml; ml.x = m; ml.y = l; *(LAS v2f*)(L + L_ML + w * 8) = ml; }
    lds_inc(L, L_FLAG + 8, lane);
  }
  STAMP(5);
  if (w >= 3) return;

  const int g32 = lane & 31, ph = lane >> 5;
  if (w == 1) {
    if (!ffn) return;
    float a0, a1, b0, b1;
    for (unsigned s = 0; s < SPIN_CAP; ++s) {
        CBAR();
      v4u G[4], U[4];
#pragma unroll
      for (int k = 0; k < 4; ++k) {
        G[k] = ld16<AUX_SC1>(rws, (ph * 1024) + g32 * 16, WS_GU + (g * 8 + 2 * k) * 1024);
        U[k] = ld16<AUX_SC1>(rws, (ph * 1024) + 512 + g32 * 16, WS_GU + (g * 8 + 2 * k) * 1024);
      }
      bool ok = true;
      a0 = a1 = b0 = b1 = 0.f;
#pragma unroll
      for (int k = 0; k < 4; ++k) {
        ok = ok && tag_ok(G[k], T) && tag_ok(U[k], T);
        a0 += asf(G[k].x); a1 += asf(G[k].z); b0 += asf(U[k].x); b1 += asf(U[k].z);
      }
      if (__all(ok)) break;
    }
    a0 = rbf(tr32(a0, a0)); a1 = rbf(tr32(a1, a1)); b0 = rbf(tr32(b0, b0)); b1 = rbf(tr32(b1, b1));
    auto gelu = [](float v) {
      const float inner = 0.7978845608028654f * (v + 0.044715f * v * v * v);
      const float th = 1.f - 2.f / (1.f + ex(2.f * inner));
      return rbf(0.5f * v * (1.f + th));
    };
    float h0 = gelu(a0) * b0, h1 = gelu(a1) * b1;
    if (64 * g + 2 * g32 >= FF) { h0 = 0.f; h1 = 0.f; }
    const unsigned hp = pk2(h0, h1);
    STAMP(6);
    lds_wait_ge(L, L_FLAG + 4, NWAVE);
    const size_t e0 = (size_t)(64 * g) * DM + 64 * j;
    const rsrc_t rd = mk(P.wd + e0, (unsigned)((size_t)FF * DM - e0) * 2u);
    v4u Wt[8];
#pragma unroll
    for (int i = 0; i < 8; ++i) Wt[i] = ld16<AUX_NT>(rd, r8 * 1024 + c8 * 16, i * 8192);
    unsigned lo[4][4], hi[4][4];
    gemv64_pre(Wt, lo, hi);
    const v2f pr = gemv64(hp, lo, hi, L + L_S + 128, lane);
    if (!(lane & 8))
      st_gran(rws, WS_S2F + (j * 32 + g) * 512 + (4 * c8 + 2 * (lane >> 5) + ((lane >> 4) & 1)) * 16, pr.x, pr.y, T);
    STAMP(7);
    return;
  }

  const int hf = w >> 1;
  const bool fin = (w == 0) && ((h & 3) == 0);
  unsigned xpair = 0;
  if (fin) xpair = ld4<0>(mk(P.x, 1024), (64 * j + 8 * (h >> 2) + 2 * (lane & 3)) * 2, 0);
  lds_wait_ge(L, L_FLAG + 4, NWAVE);
  const rsrc_t ro = mk(P.wo + (size_t)(128 * h + 64 * hf) * DM + 64 * j, 64 * 1024);
  v4u Wt[8];
#pragma unroll
  for (int i = 0; i < 8; ++i) Wt[i] = ld16<AUX_NT>(ro, r8 * 1024 + c8 * 16, i * 8192);
  SB();
  STAMP(6);
  lds_wait_ge(L, L_FLAG + 8, NWAVE);
  STAMP(7);
  {
    const v2f ml = *(LAS v2f*)(L + L_ML + (8 * ph + c8) * 8);
    v2f R[8];
#pragma unroll
    for (int k = 0; k < 8; ++k) R[k] = *(LAS v2f*)(L + L_O + (8 * ph + k) * 512 + (32 * hf + g32) * 8);
    float M = fmaxf(ml.x, dppf<XOR1>(ml.x)); M = fmaxf(M, dppf<XOR2>(M)); M = fmaxf(M, dppf<HMIR>(M));
    M = max32(M);
    const float e = ex(ml.x - M);
    float Ls = e * ml.y;
    Ls += dppf<XOR1>(Ls); Ls += dppf<XOR2>(Ls); Ls += dppf<HMIR>(Ls);
    Ls = tr32(Ls, Ls);
    float o0 = 0.f, o1 = 0.f;
#define CSTEP(k) { const float e_ = dppf<BCAST + k>(e); o0 = fmaf(e_, R[k].x, o0); o1 = fmaf(e_, R[k].y, o1); }
    CSTEP(0) CSTEP(1) CSTEP(2) CSTEP(3) CSTEP(4) CSTEP(5) CSTEP(6) CSTEP(7)
#undef CSTEP
    o0 = tr32(o0, o0); o1 = tr32(o1, o1);
    if (lane < 36) st_gran(rws, WS_OP + ((h * 8 + j) * 2 + hf) * 1024 + lane * 16, lane < 32 ? o0 : M, lane < 32 ? o1 : Ls, T);
  }
  STAMP(8);
  unsigned lo[4][4], hi[4][4];
  gemv64_pre(Wt, lo, hi);
  float on0, on1;
  for (unsigned s = 0; s < SPIN_CAP; ++s) {
        CBAR();
    v4u G[4];
#pragma unroll
    for (int k = 0; k < 4; ++k) G[k] = ld16<AUX_SC1>(rws, ph * 2048 + g32 * 16, WS_OP + ((h * 8 + 2 * k) * 2 + hf) * 1024);
    const v4u ML = ld16<AUX_SC1>(rws, (2 * (lane & 3) + ph) * 2048 + 512 + ((lane >> 2) & 3) * 16, WS_OP + (h * 8 * 2 + hf) * 1024);
    bool ok = tag_ok(ML, T);
#pragma unroll
    for (int k = 0; k < 4; ++k) ok = ok && tag_ok(G[k], T);
    const float mj = asf(ML.x), lj = asf(ML.z);
    float M = fmaxf(mj, dppf<XOR1>(mj)); M = fmaxf(M, dppf<XOR2>(M)); M = max32(M);
    const float e = ex(mj - M);
    float Ls = e * lj;
    Ls += dppf<XOR1>(Ls); Ls += dppf<XOR2>(Ls); Ls = tr32(Ls, Ls);
    const float wt = e / Ls;
    on0 = 0.f; on1 = 0.f;
#define GSTEP(k) { const float w_ = dppf<k * 0x55>(wt); on0 = fmaf(w_, asf(G[k].x), on0); on1 = fmaf(w_, asf(G[k].z), on1); }
    GSTEP(0) GSTEP(1) GSTEP(2) GSTEP(3)
#undef GSTEP
    if (__all(ok)) break;
  }
  STAMP(9);
  on0 = tr32(on0, on0); on1 = tr32(on1, on1);
  {
    const v2f pr = gemv64(pk2(on0, on1), lo, hi, L + L_S + w * 128, lane);
    if (!(lane & 8))
      st_gran(rws, WS_S2A + (j * 64 + 2 * h + hf) * 512 + (4 * c8 + 2 * (lane >> 5) + ((lane >> 4) & 1)) * 16, pr.x, pr.y, T);
  }
  STAMP(10);
  if (!fin) return;
  {
    const int kf = h >> 2;
    float s0, s1, y0, y1;
    const bool fv1 = (16 + (lane >> 2)) < NGRP;
    for (unsigned s = 0; s < SPIN_CAP; ++s) {
        CBAR();
      v4u A[4], Bf[2];
#pragma unroll
      for (int t = 0; t < 4; ++t) A[t] = ld16<AUX_SC1>(rws, (lane >> 2) * 512 + (lane & 3) * 16, WS_S2A + (j * 64 + 16 * t) * 512 + kf * 64);
#pragma unroll
      for (int t = 0; t < 2; ++t) Bf[t] = ld16<AUX_SC1>(rws, (lane >> 2) * 512 + (lane & 3) * 16, WS_S2F + (j * 32 + 16 * t) * 512 + kf * 64);
      bool ok = tag_ok(Bf[0], T) && (!fv1 || tag_ok(Bf[1], T));
      s0 = s1 = 0.f;
#pragma unroll
      for (int t = 0; t < 4; ++t) { ok = ok && tag_ok(A[t], T); s0 += asf(A[t].x); s1 += asf(A[t].z); }
      y0 = asf(Bf[0].x) + (fv1 ? asf(Bf[1].x) : 0.f);
      y1 = asf(Bf[0].z) + (fv1 ? asf(Bf[1].z) : 0.f);
      if (__all(ok)) break;
    }
    STAMP(11);
    auto red = [](float v) { v += dppf<ROR4>(v); v += dppf<ROR8>(v); v = tr16(v, v); return tr32(v, v); };
    s0 = rbf(red(s0)); s1 = rbf(red(s1)); y0 = rbf(red(y0)); y1 = rbf(red(y1));
    const float g0 = rbf(y0 / (1.f + ex(-y0))), g1 = rbf(y1 / (1.f + ex(-y1)));
    const unsigned o = pk2(lo16f(xpair) + rbf(s0 * g0), hi16f(xpair) + rbf(s1 * g1));
    if (lane < 4) ((unsigned*)P.out)[32 * j + 4 * kf + lane] = o;
    if (b == 0 && lane == 0) __builtin_amdgcn_raw_buffer_store_b32(T, rws, WS_E, 0, AUX_SC1);
    STAMP(12);
  }
}

__global__ void kA(unsigned char* ws) { if (threadIdx.x == 0) ((unsigned long long*)(ws + (3 << 20)))[0] = __builtin_amdgcn_s_memrealtime(); }
__global__ void kB(unsigned char* ws, u16* out) {
  unsigned long long t = __builtin_amdgcn_s_memrealtime();
  if (threadIdx.x == 0) { volatile unsigned long long* p = (volatile unsigned long long*)(ws + (3 << 20));
    unsigned long long a = p[0], s = p[1], e = p[2];
    unsigned long long v = LEAK == 1 ? (s - a) : LEAK == 2 ? (t - e) : LEAK == 3 ? (t - a) : (e - s);
    if (((t * 2654435761ull >> 20) & 15) == 99) out[0] = (u16)(asu((float)(v / 20)) >> 16); }
}
}

extern "C" void kernel_launch(void* const* d_in, const int* in_sizes, int n_in, void* d_out, int out_size,
                              void* d_ws, size_t ws_size, hipStream_t stream) {
  Params p;
  p.x = (const u16*)d_in[0]; p.kc = (const u16*)d_in[1]; p.vc = (const u16*)d_in[2];
  p.wq = (const u16*)d_in[3]; p.wo = (const u16*)d_in[4];
  p.wg = (const u16*)d_in[6]; p.wu = (const u16*)d_in[7]; p.wd = (const u16*)d_in[8];
  p.out = (u16*)d_out; p.ws = (unsigned char*)d_ws;
  kA<<<1, 64, 0, stream>>>(p.ws);
  fused_decode_kernel<<<NWG, NTHR, 0, stream>>>(p);
  kB<<<1, 64, 0, stream>>>(p.ws, p.out);
}
```

```cpp
#define GLATE 1
#include <hip/hip_runtime.h>
#include <hip/hip_bf16.h>
#include <stdint.h>

namespace {

typedef unsigned short u16;
typedef unsigned v4u __attribute__((ext_vector_type(4)));
typedef float v2f __attribute__((ext_vector_type(2)));
typedef __bf16 bf16x2 __attribute__((ext_vector_type(2)));
#define LAS __attribute__((address_space(3)))
typedef __amdgpu_buffer_rsrc_t rsrc_t;

constexpr int DM = 512, DH = 128, SK = 32768, WIN = 4096, FF = 1376;
constexpr int NWG = 256, NWAVE = 16, NTHR = NWAVE * 64;
constexpr int NGRP = 22;
constexpr int AUX_NT = 2, AUX_SC1 = 16;
constexpr int RW3 = 0x00020000;
constexpr int OOB = 0x7ff00000;
constexpr unsigned SPIN_CAP = 1u << 16;

constexpr int WS_E = 0;
constexpr int WS_QP = 4096;
constexpr int WS_GU = WS_QP + 32 * 8 * 1024;
constexpr int WS_OP = WS_GU + 24 * 8 * 1024;
constexpr int WS_S2A = WS_OP + 32 * 8 * 2 * 1024;
constexpr int WS_S2F = WS_S2A + 8 * 64 * 512;
constexpr int WS_END = WS_S2F + 8 * 32 * 512;

constexpr int F_Q = 0, F_QK = 4, F_REC = 8, F_QX = 12, F_VI = 20, F_LN = 24;
constexpr int L_Q = 64;
constexpr int L_XN = L_Q + 256;
constexpr int L_QX = L_XN + 4 * 1024;
constexpr int L_M = L_QX + 512;
constexpr int L_L = L_M + 64;
constexpr int L_O = L_L + 64;
constexpr int L_END = L_O + 16 * 512;

#define LN1 0
#define OUTSC1 1
#define CPRIO 1

#define SB() __builtin_amdgcn_sched_barrier(0)
#define CBAR() asm volatile("" ::: "memory")
#define PIN(x) asm volatile("" : "+v"(x))

__device__ __forceinline__ float asf(unsigned u) { return __builtin_bit_cast(float, u); }
__device__ __forceinline__ unsigned asu(float f) { return __builtin_bit_cast(unsigned, f); }
__device__ __forceinline__ float lo16f(unsigned d) { return asf(d << 16); }
__device__ __forceinline__ float hi16f(unsigned d) { return asf(d & 0xffff0000u); }
__device__ __forceinline__ unsigned pk2(float lo, float hi) {
  bf16x2 r = {(__bf16)lo, (__bf16)hi};
  return __builtin_bit_cast(unsigned, r);
}
__device__ __forceinline__ float rbf(float f) { return lo16f(pk2(f, f)); }
__device__ __forceinline__ float dot2(unsigned a, unsigned b, float c) {
  return __builtin_amdgcn_fdot2_f32_bf16(__builtin_bit_cast(bf16x2, a), __builtin_bit_cast(bf16x2, b), c, false);
}
template <int CTRL> __device__ __forceinline__ unsigned dppu(unsigned x) { return __builtin_amdgcn_mov_dpp(x, CTRL, 0xf, 0xf, true); }
template <int CTRL> __device__ __forceinline__ float dppf(float x) { return asf(dppu<CTRL>(asu(x))); }
constexpr int XOR1 = 0xB1, XOR2 = 0x4E, XOR3 = 0x1B, HMIR = 0x141, ROR1 = 0x121, ROR2 = 0x122, ROR4 = 0x124, ROR8 = 0x128, BCAST = 0x150;
__device__ __forceinline__ float tr32(float a, float b) {
  auto s = __builtin_amdgcn_permlane32_swap(asu(a), asu(b), false, false);
  return asf(s[0]) + asf(s[1]);
}
__device__ __forceinline__ float tr16(float a, float b) {
  auto s = __builtin_amdgcn_permlane16_swap(asu(a), asu(b), false, false);
  return asf(s[0]) + asf(s[1]);
}
__device__ __forceinline__ float max32(float a) {
  auto s = __builtin_amdgcn_permlane32_swap(asu(a), asu(a), false, false);
  return fmaxf(asf(s[0]), asf(s[1]));
}
__device__ __forceinline__ float max16(float a) {
  auto s = __builtin_amdgcn_permlane16_swap(asu(a), asu(a), false, false);
  return fmaxf(asf(s[0]), asf(s[1]));
}
__device__ __forceinline__ float row_sum(float x) { x += dppf<ROR8>(x); x += dppf<ROR4>(x); x += dppf<ROR2>(x); return x + dppf<ROR1>(x); }
__device__ __forceinline__ float row_max(float x) {
  x = fmaxf(x, dppf<ROR8>(x)); x = fmaxf(x, dppf<ROR4>(x)); x = fmaxf(x, dppf<ROR2>(x)); return fmaxf(x, dppf<ROR1>(x));
}
__device__ __forceinline__ float wave_sum(float x) { x = row_sum(x); x = tr16(x, x); return tr32(x, x); }
__device__ __forceinline__ float xor4(float x) { return dppf<XOR3>(dppf<HMIR>(x)); }
__device__ __forceinline__ float ex(float x) { return __builtin_amdgcn_exp2f(x * 1.4426950408889634f); }

__device__ __forceinline__ rsrc_t mk(const void* p, unsigned bytes) { return __builtin_amdgcn_make_buffer_rsrc((void*)p, 0, bytes, RW3); }
template <int AUX> __device__ __forceinline__ v4u ld16(rsrc_t r, int voff, int soff) { return __builtin_amdgcn_raw_buffer_load_b128(r, voff, soff, AUX); }
template <int AUX> __device__ __forceinline__ unsigned ld4(rsrc_t r, int voff, int soff) { return __builtin_amdgcn_raw_buffer_load_b32(r, voff, soff, AUX); }
__device__ __forceinline__ void st_gran(rsrc_t r, int voff, float a, float b, unsigned T) {
  v4u g = {asu(a), T, asu(b), ~T};
  __builtin_amdgcn_raw_buffer_store_b128(g, r, voff, 0, AUX_SC1);
}
__device__ __forceinline__ bool tag_ok(v4u g, unsigned T) { return (g.y == T) & (g.w == ~T); }

__device__ __forceinline__ unsigned lds_ld(LAS unsigned char* L, int off) { return *(volatile LAS unsigned*)(L + off); }
template <bool SLEEP> __device__ __forceinline__ void lds_wait(LAS unsigned char* L, int off, unsigned want) {
  for (unsigned s = 0; s < (SPIN_CAP << 6); ++s) {
    if ((unsigned)__builtin_amdgcn_readfirstlane(lds_ld(L, off)) >= want) break;
    if (SLEEP) __builtin_amdgcn_s_sleep(1);
  }
  CBAR();
}
__device__ __forceinline__ void lds_inc(LAS unsigned char* L, int off, int lane) {
  CBAR();
  if (lane == 0) __hip_atomic_fetch_add((LAS unsigned*)(L + off), 1u, __ATOMIC_RELAXED, __HIP_MEMORY_SCOPE_WORKGROUP);
  CBAR();
}

#define STAMP(i) do { } while (0)
#define PSEUDO(i, v) do { } while (0)
#define DUMP() do { } while (0)

__device__ __forceinline__ void layernorm_to_lds(v4u xv, LAS unsigned char* xb, int lane) {
  float f[8];
#pragma unroll
  for (int k = 0; k < 4; ++k) { f[2 * k] = lo16f(xv[k]); f[2 * k + 1] = hi16f(xv[k]); }
  float s = 0.f;
#pragma unroll
  for (int k = 0; k < 8; ++k) s += f[k];
  const float mean = wave_sum(s) * (1.f / 512.f);
  float v = 0.f;
#pragma unroll
  for (int k = 0; k < 8; ++k) { const float d = f[k] - mean; v = fmaf(d, d, v); }
  const float var = rbf(wave_sum(v) * (1.f / 512.f));
  const float m = rbf(mean);
  const float den = rbf(__builtin_amdgcn_sqrtf(rbf(var + 1e-5f)));
  const float inv = __builtin_amdgcn_rcpf(den);
  v4u o;
#pragma unroll
  for (int k = 0; k < 4; ++k) {
    const float a0 = rbf(f[2 * k] - m), a1 = rbf(f[2 * k + 1] - m);
    float q0 = a0 * inv, q1 = a1 * inv;
    q0 = fmaf(fmaf(-q0, den, a0), inv, q0); q1 = fmaf(fmaf(-q1, den, a1), inv, q1);
    o[k] = pk2(q0, q1);
  }
  *(LAS v4u*)(xb + lane * 16) = o;
}

__device__ __forceinline__ v2f reduce_r8(const float (&acc)[8]) {
  float t[4], u[2];
#pragma unroll
  for (int e = 0; e < 4; ++e) t[e] = tr32(acc[e], acc[e + 4]);
#pragma unroll
  for (int e = 0; e < 2; ++e) u[e] = tr16(t[e], t[e + 2]);
  v2f r; r.x = u[0] + dppf<ROR8>(u[0]); r.y = u[1] + dppf<ROR8>(u[1]);
  return r;
}
__device__ __forceinline__ v2f reduce_r4(const float (&acc)[8]) {
  float t[4];
#pragma unroll
  for (int e = 0; e < 4; ++e) t[e] = tr32(acc[e], acc[e + 4]);
  v2f r; r.x = tr16(t[0], t[2]); r.y = tr16(t[1], t[3]);
  return r;
}

__device__ __forceinline__ void load_w64(v4u (&Wt)[8], rsrc_t r, int lane) {
#pragma unroll
  for (int i = 0; i < 8; ++i) Wt[i] = ld16<AUX_NT>(r, (lane >> 3) * 2048 + (lane & 7) * 16, (16 * (i >> 1) + (i & 1)) * 1024);
}
__device__ __forceinline__ void gemv_pre(const v4u (&Wt)[8], unsigned (&lo)[4][4], unsigned (&hi)[4][4]) {
#pragma unroll
  for (int t = 0; t < 4; ++t) {
#pragma unroll
    for (int k = 0; k < 4; ++k) {
      lo[t][k] = __builtin_amdgcn_perm(Wt[2 * t + 1][k], Wt[2 * t][k], 0x05040100u);
      hi[t][k] = __builtin_amdgcn_perm(Wt[2 * t + 1][k], Wt[2 * t][k], 0x07060302u);
    }
    asm volatile("" : "+v"(lo[t][0]), "+v"(lo[t][1]), "+v"(lo[t][2]), "+v"(lo[t][3]), "+v"(hi[t][0]), "+v"(hi[t][1]), "+v"(hi[t][2]), "+v"(hi[t][3]));
  }
}
__device__ __forceinline__ v2f gemv64(unsigned P, const unsigned (&lo)[4][4], const unsigned (&hi)[4][4]) {
  float acc[8];
#pragma unroll
  for (int k = 0; k < 8; ++k) acc[k] = 0.f;
#define GV(t) { const unsigned D_ = dppu<t * 0x55>(P); _Pragma("unroll") for (int k = 0; k < 4; ++k) { \
    acc[2 * k] = dot2(lo[t][k], D_, acc[2 * k]); acc[2 * k + 1] = dot2(hi[t][k], D_, acc[2 * k + 1]); } }
  GV(0) GV(1) GV(2) GV(3)
#undef GV
  return reduce_r8(acc);
}
template <int I> __device__ __forceinline__ void axpy8(float (&acc)[8], v4u A, unsigned slo, unsigned shi) {
  const unsigned a = dppu<BCAST + I>(slo), b = dppu<BCAST + I>(shi);
#pragma unroll
  for (int k = 0; k < 4; ++k) { acc[2 * k] = dot2(A[k], a, acc[2 * k]); acc[2 * k + 1] = dot2(A[k], b, acc[2 * k + 1]); }
}

struct Params {
  const u16* x; const u16* kc; const u16* vc; const u16* wq; const u16* wo;
  const u16* wg; const u16* wu; const u16* wd; u16* out; unsigned char* ws;
};

__global__ void __launch_bounds__(NTHR, 4) fused_decode_kernel(Params P) {
  __shared__ __attribute__((aligned(16))) unsigned char lds_raw[L_END];
  LAS unsigned char* L = (LAS unsigned char*)lds_raw;
  const int tid = threadIdx.x, lane = tid & 63;
  const int w = __builtin_amdgcn_readfirstlane(tid >> 6);
  const int b = blockIdx.x, xcd = b & 7, idx = b >> 3;
  const int h = xcd * 4 + (idx >> 3), j = idx & 7;
  const int g = (idx >> 3) * 8 + xcd;
  const bool ffn = g < NGRP;
  const int r = lane >> 4, c = lane & 15, r8 = lane >> 3, c8 = lane & 7;
  STAMP(0);
  if (tid < 8) ((LAS unsigned*)L)[tid] = 0u;
  asm volatile("s_waitcnt lgkmcnt(0)" ::: "memory");
  if (w >= 4) __builtin_amdgcn_s_barrier();
  CBAR();
  __builtin_amdgcn_s_setprio(2);
  const rsrc_t rws = mk(P.ws, WS_END);
  unsigned T = 0;

  if (w < 4) {
    unsigned E = ld4<AUX_SC1>(rws, WS_E, 0);
    if (w == 0 || w == 3) {
      const int hq = w == 0 ? 0 : 1;
      v4u xv;
      if (!LN1 || w == 3) xv = ld16<0>(mk(P.x, 1024), lane * 16, 0);
      const rsrc_t rq = mk(P.wq + (size_t)(64 * j + 32 * hq) * 4096 + 128 * h, 32 * 8192);
      v4u A[8];
#pragma unroll
      for (int i = 0; i < 8; ++i) A[i] = ld16<AUX_NT>(rq, r * 4 * 8192 + c * 16, ((i & 3) + 16 * (i >> 2)) * 8192);
      SB(); __builtin_amdgcn_s_barrier(); SB();
      PIN(E);
      T = __builtin_amdgcn_readfirstlane(E) + 1u;
      LAS unsigned char* xb = L + L_XN + (LN1 ? 0 : w * 1024);
      if (!LN1 || w == 3) layernorm_to_lds(xv, xb, lane);
      if (LN1) { if (w == 3) lds_inc(L, F_LN, lane); else lds_wait<false>(L, F_LN, 1u); }
      const unsigned slo = *(LAS u16*)(xb + (64 * j + 32 * hq + (c & 3) + 4 * r + 16 * ((c >> 2) & 1)) * 2), shi = slo << 16;
      float acc[8];
#pragma unroll
      for (int k = 0; k < 8; ++k) acc[k] = 0.f;
      axpy8<0>(acc, A[0], slo, shi); axpy8<1>(acc, A[1], slo, shi); axpy8<2>(acc, A[2], slo, shi); axpy8<3>(acc, A[3], slo, shi);
      axpy8<4>(acc, A[4], slo, shi); axpy8<5>(acc, A[5], slo, shi); axpy8<6>(acc, A[6], slo, shi); axpy8<7>(acc, A[7], slo, shi);
      const v2f u = reduce_r4(acc);
      if (w == 3) {
        *(LAS v2f*)(L + L_QX + lane * 8) = u;
        lds_inc(L, F_QX, lane);
      } else {
        lds_wait<true>(L, F_QX, 1u);
        const v2f o = *(LAS v2f*)(L + L_QX + lane * 8);
        st_gran(rws, WS_QP + (h * 8 + j) * 1024 + (4 * c + r) * 16, u.x + o.x, u.y + o.y, T);
      }
    } else if (ffn) {
      v4u xv;
      if (!LN1) xv = ld16<0>(mk(P.x, 1024), lane * 16, 0);
      const int par = j >> 2;
      const size_t e0 = (size_t)(128 * (j & 3) + par) * FF + 64 * g - 32 * par;
      const rsrc_t rg = mk((w == 1 ? P.wg : P.wu) + e0, (unsigned)((size_t)DM * FF - e0) * 2u);
      v4u A[8];
#pragma unroll
      for (int i = 0; i < 8; ++i) A[i] = ld16<AUX_NT>(rg, r8 * (FF * 4) + c8 * 16, i * 8 * FF * 4);
      SB(); __builtin_amdgcn_s_barrier(); SB();
      PIN(E);
      T = __builtin_amdgcn_readfirstlane(E) + 1u;
      LAS unsigned char* xb = L + L_XN + (LN1 ? 0 : w * 1024);
      if (!LN1) layernorm_to_lds(xv, xb, lane); else lds_wait<false>(L, F_LN, 1u);
      float acc[8];
#pragma unroll
      for (int k = 0; k < 8; ++k) acc[k] = 0.f;
#pragma unroll
      for (int i = 0; i < 8; ++i) {
        const unsigned slo = *(LAS u16*)(xb + (128 * (j & 3) + 2 * (8 * i + r8) + par) * 2), shi = slo << 16;
#pragma unroll
        for (int k = 0; k < 4; ++k) { acc[2 * k] = dot2(A[i][k], slo, acc[2 * k]); acc[2 * k + 1] = dot2(A[i][k], shi, acc[2 * k + 1]); }
      }
      const v2f pr = reduce_r8(acc);
      if (!(lane & 8)) st_gran(rws, WS_GU + (g * 8 + j) * 1024 + (w - 1) * 512 + (4 * c8 + 2 * (lane >> 5) + ((lane >> 4) & 1)) * 16, pr.x, pr.y, T);
    } else {
      SB(); __builtin_amdgcn_s_barrier(); SB();
      PIN(E);
      T = __builtin_amdgcn_readfirstlane(E) + 1u;
    }
  }
  STAMP(1);

  v2f o2;
  float m, l, f, Mcu;
  unsigned cnt;
  {
    const size_t base = ((size_t)h * SK + (SK - WIN) + j * 512) * DH;
    const rsrc_t rk = mk(P.kc + base, 512 * 256), rv = mk(P.vc + base, 512 * 256);
    const int cx = (b * 37) & 127;
    const int voff = ((((w ^ cx) & 15) | (r << 7)) << 8) + c * 16;
    const int ix = cx >> 4;
    v4u K[8], V[8];
#pragma unroll
    for (int i = 0; i < 8; ++i) K[i] = ld16<AUX_NT>(rk, voff, ((i ^ ix) & 7) << 12);
    SB();
    if (w == 0) {
      v4u G[8];
      unsigned qrounds = 0;
      for (unsigned s = 0; s < SPIN_CAP; ++s) {
        qrounds++;
        CBAR();
#pragma unroll
        for (int k = 0; k < 8; ++k) G[k] = ld16<AUX_SC1>(rws, lane * 16, WS_QP + (h * 8 + k) * 1024);
        bool all = true;
#pragma unroll
        for (int k = 0; k < 8; ++k) all = all & tag_ok(G[k], T);
        if (__all(all)) break;
      }
      float q0 = 0.f, q1 = 0.f;
#pragma unroll
      for (int k = 0; k < 8; ++k) { q0 += asf(G[k].x); q1 += asf(G[k].z); }
      *(LAS unsigned*)(L + L_Q + lane * 4) = pk2(q0, q1);
      lds_inc(L, F_Q, lane);
      PSEUDO(11, qrounds);
    }
    SB(); __builtin_amdgcn_s_setprio(0); SB();
#pragma unroll
    for (int i = 0; i < 8; ++i) V[i] = ld16<AUX_NT>(rv, voff, ((i ^ ix) & 7) << 12);
    SB();
    if (CPRIO) {
      if (w >= 12) __builtin_amdgcn_s_setprio(3); else if (w >= 8) __builtin_amdgcn_s_setprio(2); else if (w >= 4) __builtin_amdgcn_s_setprio(1);
      SB();
    }
    lds_inc(L, F_VI, lane);
    STAMP(2);
    lds_wait<true>(L, F_Q, 1u);
    const v4u q = *(LAS v4u*)(L + L_Q + c * 16);
    STAMP(3);
    float x[8];
#pragma unroll
    for (int i = 0; i < 8; ++i) {
      float d = 0.f;
#pragma unroll
      for (int k = 0; k < 4; ++k) d = dot2(K[i][k], q[k], d);
      x[i] = d;
    }
    float y[4], z[2];
#pragma unroll
    for (int i = 0; i < 4; ++i) { const float t1 = x[i] + dppf<ROR8>(x[i]), t2 = x[i + 4] + dppf<ROR8>(x[i + 4]); y[i] = (c & 8) ? t2 : t1; }
#pragma unroll
    for (int i = 0; i < 2; ++i) { const float t1 = y[i] + dppf<HMIR>(y[i]), t2 = y[i + 2] + dppf<HMIR>(y[i + 2]); z[i] = (c & 4) ? t2 : t1; }
    const float t1 = z[0] + dppf<XOR2>(z[0]), t2 = z[1] + dppf<XOR2>(z[1]);
    const float u = (c & 2) ? t2 : t1;
    const float sc = rbf(rbf(u + dppf<XOR1>(u)) * 0.08838834764831845f);
    m = fmaxf(sc, dppf<ROR8>(sc)); m = fmaxf(m, dppf<ROR4>(m)); m = fmaxf(m, dppf<ROR2>(m));
    m = max32(max16(m));
    if (lane == 0) *(LAS float*)(L + L_M + w * 4) = m;
    lds_inc(L, F_QK, lane);
    cnt = lds_ld(L, F_QK);
    const float mall = *(volatile LAS float*)(L + L_M + c * 4);
    const float p = ex(sc - m);
    l = wave_sum((c & 1) ? 0.f : p);
    const unsigned shi = asu(rbf(p)), slo = shi >> 16;
    STAMP(4);
    Mcu = row_max(mall);
    f = ex(m - Mcu);
    float acc[8];
#pragma unroll
    for (int k = 0; k < 8; ++k) acc[k] = 0.f;
    axpy8<0>(acc, V[0], slo, shi); axpy8<2>(acc, V[1], slo, shi); axpy8<4>(acc, V[2], slo, shi); axpy8<6>(acc, V[3], slo, shi);
    axpy8<8>(acc, V[4], slo, shi); axpy8<10>(acc, V[5], slo, shi); axpy8<12>(acc, V[6], slo, shi); axpy8<14>(acc, V[7], slo, shi);
    o2 = reduce_r4(acc);
  }
  auto post_record = [&]() {
    if ((unsigned)__builtin_amdgcn_readfirstlane(cnt) < (unsigned)NWAVE) {
      lds_wait<true>(L, F_QK, NWAVE);
      Mcu = row_max(*(volatile LAS float*)(L + L_M + c * 4));
      f = ex(m - Mcu);
    }
    if (lane == 0) *(LAS float*)(L + L_L + w * 4) = l * f;
    v2f o; o.x = o2.x * f; o.y = o2.y * f;
    *(LAS v2f*)(L + L_O + w * 512 + (4 * c + r) * 8) = o;
    lds_inc(L, F_REC, lane);
  };
  const int gidx = (4 * c8 + 2 * (lane >> 5) + ((lane >> 4) & 1)) * 16;

  if (w == 3 && ffn) {
    const int fu = 8 * (lane & 3) + r8, p2 = (lane >> 2) & 1;
    const int vo0 = p2 * 1024 + fu * 16;
    const bool skip = fu >= 16 && g + 1 >= NGRP;
    const int vo1 = skip ? OOB : (4 + p2) * 1024 + (fu >= 16 ? 8 * 1024 + (fu - 16) * 16 : (16 + fu) * 16);
    v4u G[8], Wt[8];
#define FLOAD(k) ld16<AUX_SC1>(rws, ((k) < 4 ? vo0 : vo1) + ((k) & 1) * 512, WS_GU + (g * 8 + 2 * (((k) >> 1) & 1)) * 1024)
    PIN(o2.x); PIN(o2.y);
    if (GLATE) lds_wait<true>(L, F_VI, NWAVE);
#pragma unroll
    for (int k = 0; k < 8; ++k) G[k] = FLOAD(k);
    if (!GLATE) { SB(); lds_wait<true>(L, F_VI, NWAVE); }
    const size_t e0 = (size_t)(64 * g) * DM + 64 * j;
    load_w64(Wt, mk(P.wd + e0, (unsigned)((size_t)FF * DM - e0) * 2u), lane);
    SB();
    post_record();
    STAMP(5);
    for (unsigned s = 0; s < SPIN_CAP; ++s) {
      bool all = true;
#pragma unroll
      for (int k = 0; k < 8; ++k) all = all & (bool)(tag_ok(G[k], T) | (k >= 4 && skip));
      if (__all(all)) break;
      CBAR();
#pragma unroll
      for (int k = 0; k < 8; ++k) G[k] = FLOAD(k);
    }
#undef FLOAD
    float a0 = 0.f, a1 = 0.f, b0 = 0.f, b1 = 0.f;
#pragma unroll
    for (int k = 0; k < 4; ++k) { a0 += asf(G[2 * k].x); a1 += asf(G[2 * k].z); b0 += asf(G[2 * k + 1].x); b1 += asf(G[2 * k + 1].z); }
    a0 = rbf(a0 + xor4(a0)); a1 = rbf(a1 + xor4(a1)); b0 = rbf(b0 + xor4(b0)); b1 = rbf(b1 + xor4(b1));
    auto gelu = [](float v) {
      const float inner = 0.7978845608028654f * (v + 0.044715f * v * v * v);
      const float th = 1.f - 2.f * __builtin_amdgcn_rcpf(1.f + ex(2.f * inner));
      return rbf(0.5f * v * (1.f + th));
    };
    float h0 = gelu(a0) * b0, h1 = gelu(a1) * b1;
    if (64 * g + 2 * fu >= FF) { h0 = 0.f; h1 = 0.f; }
    unsigned lo[4][4], hi[4][4];
    gemv_pre(Wt, lo, hi);
    const v2f pr = gemv64(pk2(h0, h1), lo, hi);
    if (!(lane & 8)) st_gran(rws, WS_S2F + (j * 32 + g) * 512 + gidx, pr.x, pr.y, T);
    STAMP(7);
    DUMP();
    return;
  }
  if (w != 1 && w != 2) {
    post_record();
    STAMP(5);
    DUMP();
    return;
  }

  const int hf = w - 1;
  const bool fin = w == 1 && (h & 3) == 0;
  v4u Wt[8];
  PIN(o2.x); PIN(o2.y);
  lds_wait<true>(L, F_VI, NWAVE);
  load_w64(Wt, mk(P.wo + (size_t)(128 * h + 64 * hf) * DM + 64 * j, 64 * 1024), lane);
  SB();
  post_record();
  STAMP(5);
  unsigned xpair = 0;
  if (fin) xpair = ld4<0>(mk(P.x, 1024), (64 * j + 8 * (h >> 2) + 2 * (lane & 3)) * 2, 0);
  {
    int ra = L_O + 8 * (lane >> 5) * 512 + (32 * hf + (lane & 31)) * 8, la = L_L + c * 4;
    int so = WS_OP + ((h * 8 + j) * 2 + hf) * 1024 + lane * 16;
    PIN(ra); PIN(la); PIN(so);
    STAMP(6);
    lds_wait<false>(L, F_REC, NWAVE);
    STAMP(7);
    v2f R[8];
#pragma unroll
    for (int k = 0; k < 8; ++k) R[k] = *(LAS v2f*)(L + ra + k * 512);
    const float Ls = row_sum(*(LAS float*)(L + la));
    float o0 = ((R[0].x + R[1].x) + (R[2].x + R[3].x)) + ((R[4].x + R[5].x) + (R[6].x + R[7].x));
    float o1 = ((R[0].y + R[1].y) + (R[2].y + R[3].y)) + ((R[4].y + R[5].y) + (R[6].y + R[7].y));
    o0 = tr32(o0, o0); o1 = tr32(o1, o1);
    if (lane < 36) st_gran(rws, so, lane < 32 ? o0 : Mcu, lane < 32 ? o1 : Ls, T);
  }
  STAMP(8);
  unsigned lo[4][4], hi[4][4];
  unsigned Pp;
  {
    const int p2 = (lane >> 2) & 1;
    const int vo = p2 * 2048 + (8 * (lane & 3) + r8) * 16, vm = (2 * (lane & 3) + p2) * 2048 + 512 + (r8 & 3) * 16;
    const int so = WS_OP + (h * 8 * 2 + hf) * 1024;
    v4u G[5];
#pragma unroll
    for (int k = 0; k < 4; ++k) G[k] = ld16<AUX_SC1>(rws, vo, so + k * 4096);
    G[4] = ld16<AUX_SC1>(rws, vm, so);
    SB();
    gemv_pre(Wt, lo, hi);
    SB();
    STAMP(13);
    unsigned rounds = 0;
    for (unsigned s = 0; s < SPIN_CAP; ++s) {
      rounds++;
      bool all = true;
      bool ok[5];
#pragma unroll
      for (int k = 0; k < 5; ++k) { ok[k] = tag_ok(G[k], T); all = all & ok[k]; }
      if (__all(all)) break;
      CBAR();
      v4u N[5];
#pragma unroll
      for (int k = 0; k < 4; ++k) N[k] = ld16<AUX_SC1>(rws, ok[k] ? OOB : vo, so + k * 4096);
      N[4] = ld16<AUX_SC1>(rws, ok[4] ? OOB : vm, so);
#pragma unroll
      for (int k = 0; k < 5; ++k) G[k] = ok[k] ? G[k] : N[k];
    }
    PSEUDO(14, rounds);
    const float mj = asf(G[4].x), lj = asf(G[4].z);
    float M = fmaxf(mj, dppf<XOR1>(mj)); M = fmaxf(M, dppf<XOR2>(M)); M = fmaxf(M, dppf<HMIR>(M));
    const float e = ex(mj - M);
    float Ls = e * lj;
    Ls += dppf<XOR1>(Ls); Ls += dppf<XOR2>(Ls); Ls += dppf<HMIR>(Ls);
    const float inv = __builtin_amdgcn_rcpf(Ls);
    const float e0 = dppf<0x00>(e), e1 = dppf<0x55>(e), e2 = dppf<0xAA>(e), e3 = dppf<0xFF>(e);
    const float on0 = fmaf(e0, asf(G[0].x), e1 * asf(G[1].x)) + fmaf(e2, asf(G[2].x), e3 * asf(G[3].x));
    const float on1 = fmaf(e0, asf(G[0].z), e1 * asf(G[1].z)) + fmaf(e2, asf(G[2].z), e3 * asf(G[3].z));
    Pp = pk2((on0 + xor4(on0)) * inv, (on1 + xor4(on1)) * inv);
  }
  STAMP(9);
  {
    const v2f pr = gemv64(Pp, lo, hi);
    if (!(lane & 8)) st_gran(rws, WS_S2A + (j * 64 + 2 * h + hf) * 512 + gidx, pr.x, pr.y, T);
  }
  STAMP(10);
  if (!fin) { DUMP(); return; }

  {
    const int kf = h >> 2;
    const int vo = (lane >> 2) * 512 + (lane & 3) * 16;
    const int sa = WS_S2A + j * 64 * 512 + kf * 64, sf = WS_S2F + j * 32 * 512 + kf * 64;
    const bool fv1 = (16 + (lane >> 2)) < NGRP;
    auto red = [](float v) { v += dppf<ROR4>(v); v += dppf<ROR8>(v); v = tr16(v, v); return tr32(v, v); };
    v4u A[4], B[2];
#pragma unroll
    for (int t = 0; t < 2; ++t) B[t] = ld16<AUX_SC1>(rws, (t == 0 || fv1) ? vo : OOB, sf + t * 16 * 512);
#pragma unroll
    for (int t = 0; t < 4; ++t) A[t] = ld16<AUX_SC1>(rws, vo, sa + t * 16 * 512);
    for (unsigned s = 0; s < SPIN_CAP; ++s) {
      const bool ok0 = tag_ok(B[0], T), ok1 = (bool)(!fv1 | tag_ok(B[1], T));
      if (__all(ok0 & ok1)) break;
      CBAR();
      const v4u N0 = ld16<AUX_SC1>(rws, ok0 ? OOB : vo, sf), N1 = ld16<AUX_SC1>(rws, ok1 ? OOB : vo, sf + 16 * 512);
      B[0] = ok0 ? B[0] : N0; B[1] = ok1 ? B[1] : N1;
    }
    const float y0 = rbf(red(asf(B[0].x) + (fv1 ? asf(B[1].x) : 0.f))), y1 = rbf(red(asf(B[0].z) + (fv1 ? asf(B[1].z) : 0.f)));
    float g0 = rbf(y0 / (1.f + ex(-y0))), g1 = rbf(y1 / (1.f + ex(-y1)));
    float x0 = lo16f(xpair), x1 = hi16f(xpair);
    int oo = (32 * j + 4 * kf + lane) * 4;
    PIN(g0); PIN(g1); PIN(x0); PIN(x1); PIN(oo);
    for (unsigned s = 0; s < SPIN_CAP; ++s) {
      bool all = true;
      bool ok[4];
#pragma unroll
      for (int t = 0; t < 4; ++t) { ok[t] = tag_ok(A[t], T); all = all & ok[t]; }
      if (__all(all)) break;
      CBAR();
      v4u N[4];
#pragma unroll
      for (int t = 0; t < 4; ++t) N[t] = ld16<AUX_SC1>(rws, ok[t] ? OOB : vo, sa + t * 16 * 512);
#pragma unroll
      for (int t = 0; t < 4; ++t) A[t] = ok[t] ? A[t] : N[t];
    }
    STAMP(11);
    const float s0 = rbf(red((asf(A[0].x) + asf(A[1].x)) + (asf(A[2].x) + asf(A[3].x))));
    const float s1 = rbf(red((asf(A[0].z) + asf(A[1].z)) + (asf(A[2].z) + asf(A[3].z))));
    const unsigned o = pk2(x0 + rbf(s0 * g0), x1 + rbf(s1 * g1));
    if (lane < 4) __builtin_amdgcn_raw_buffer_store_b32(o, mk(P.out, 1024), oo, 0, OUTSC1 ? AUX_SC1 : 0);
    if (b == 0 && lane == 0) __builtin_amdgcn_raw_buffer_store_b32(T, rws, WS_E, 0, AUX_SC1);
    STAMP(12);
    DUMP();
  }
}

}

extern "C" void kernel_launch(void* const* d_in, const int* in_sizes, int n_in, void* d_out, int out_size,
                              void* d_ws, size_t ws_size, hipStream_t stream) {
  Params p;
  p.x = (const u16*)d_in[0]; p.kc = (const u16*)d_in[1]; p.vc = (const u16*)d_in[2];
  p.wq = (const u16*)d_in[3]; p.wo = (const u16*)d_in[4];
  p.wg = (const u16*)d_in[6]; p.wu = (const u16*)d_in[7]; p.wd = (const u16*)d_in[8];
  p.out = (u16*)d_out; p.ws = (unsigned char*)d_ws;
  fused_decode_kernel<<<NWG, NTHR, 0, stream>>>(p);
}
```
